```python
import torch

def _softmax(x, axis=-1):
    return torch.softmax(x, dim=axis)
def _gelu(x):
    return torch.nn.functional.gelu(x, approximate="tanh")
def _silu(x):
    return torch.nn.functional.silu(x)
def _sigmoid(x):
    return torch.sigmoid(x)
def _layernorm(x, axis=-1, eps=1e-5):
    m = x.mean(dim=axis, keepdim=True)
    v = x.var(dim=axis, keepdim=True, unbiased=False)
    return (x - m) / torch.sqrt(v + eps)
def _scatter_add(tgt, idx, src, axis=0):
    return tgt.clone().index_add_(axis, idx, src)
def _segment_sum(src, seg, n):
    out = torch.zeros((n,) + src.shape[1:], dtype=src.dtype)
    return out.index_add_(0, seg, src)
def _pad(x, pw):
    flat = tuple(v for pair in reversed(pw) for v in pair)
    return torch.nn.functional.pad(x, flat)

kernel_name = 'sequential_preLN__sliding_block_cached__glu_gated_swiglu__decode'

def setup_inputs():

    _w = (lambda a: (lambda t: t.to(torch.bfloat16) if t.is_floating_point() else t)(torch.as_tensor(a)))
    def _A_setup():
        B, Sq, D = 4, 1, 640
        Hq, Hkv, Sk, Dh = 32, 32, 8192, 128
        assert Hq % Hkv == 0, (Hq, Hkv)
        return {
            "x": _w((lambda *s: torch.randn(*s, dtype=torch.bfloat16) * 0.05)(B, Sq, D)),
            "k_cache": _w((lambda *s: torch.randn(*s, dtype=torch.bfloat16) * 1.6)(B, Hkv, Sk, Dh)),
            "v_cache": _w((lambda *s: torch.randn(*s, dtype=torch.bfloat16) * 0.8)(B, Hkv, Sk, Dh)),
            "wq": _w((lambda *s: torch.randn(*s, dtype=torch.bfloat16) * 1.6)(D, Hq * Dh) * (D ** -0.5)),
            "wo": _w((lambda *s: torch.randn(*s, dtype=torch.bfloat16) * 0.8)(Hq * Dh, D) * ((Hq * Dh) ** -0.5)),
        }
    _A_in = _A_setup()

    def _B_setup():
        B, S, D, F = 4, 1, 640, 1728
        return {
            "x": _w((lambda *s: torch.randn(*s, dtype=torch.bfloat16) * 0.05)(B, S, D)),
            "w1": _w((lambda *s: torch.randn(*s, dtype=torch.bfloat16) * 0.4)(D, F) * (2.0 / D) ** 0.5),
            "w3": _w((lambda *s: torch.randn(*s, dtype=torch.bfloat16) * 0.4)(D, F) * (2.0 / D) ** 0.5),
            "w2": _w((lambda *s: torch.randn(*s, dtype=torch.bfloat16) * 0.4)(F, D) * (2.0 / F) ** 0.5),
        }
    _B_in = _B_setup()

    return {
        "A_x": _A_in["x"],
        "A_k_cache": _A_in["k_cache"],
        "A_v_cache": _A_in["v_cache"],
        "A_wq": _A_in["wq"],
        "A_wo": _A_in["wo"],
        "B_x": _B_in["x"],
        "B_w1": _B_in["w1"],
        "B_w3": _B_in["w3"],
        "B_w2": _B_in["w2"],
    }

def reference(A_x, A_k_cache, A_v_cache, A_wq, A_wo, B_x, B_w1, B_w3, B_w2):
    def _A_ref(x, k_cache, v_cache, wq, wo):
        B, Sq, D = x.shape
        Hq, Dh = 32, 128
        Hkv, Sk = k_cache.shape[1], k_cache.shape[2]
        W = min(4096, max(256, Sk // 4))
        rep = Hq // Hkv
        q = (lambda x, *d: x.permute(*d))((x @ wq).reshape(B, Sq, Hq, Dh), 0, 2, 1, 3)
        k = (lambda x, n, axis: torch.repeat_interleave(x, n, dim=axis))(k_cache[:, :, Sk - W:, :], rep, axis=1)
        v = (lambda x, n, axis: torch.repeat_interleave(x, n, dim=axis))(v_cache[:, :, Sk - W:, :], rep, axis=1)
        scores = torch.einsum("bhqd,bhkd->bhqk", q, k) * (Dh ** -0.5)
        p = _softmax(scores, axis=-1)
        o = torch.einsum("bhqk,bhkd->bhqd", p, v)
        return (lambda x, *d: x.permute(*d))(o, 0, 2, 1, 3).reshape(B, Sq, Hq * Dh) @ wo

    def _B_ref(x, w1, w3, w2):
        gate = torch.einsum("bsd,df->bsf", x, w1)
        up = torch.einsum("bsd,df->bsf", x, w3)
        h = _silu(gate) * up
        out = torch.einsum("bsf,fd->bsd", h, w2)
        return out

    x = A_x
    h = x + _A_ref(_layernorm(x, axis=-1), A_k_cache, A_v_cache, A_wq, A_wo)
    h2 = h + _B_ref(_layernorm(h, axis=-1), B_w1, B_w3, B_w2)
    return h2
```

```cpp
#include <hip/hip_runtime.h>
#include <hip/hip_bf16.h>
#include <stdint.h>

namespace fk {
#define GAS __attribute__((address_space(1)))
#define LAS __attribute__((address_space(3)))
typedef unsigned v4u __attribute__((ext_vector_type(4)));
typedef unsigned v2u __attribute__((ext_vector_type(2)));
typedef float f32x4 __attribute__((ext_vector_type(4)));
typedef short s16x4 __attribute__((ext_vector_type(4)));
typedef __bf16 bf16x8 __attribute__((ext_vector_type(8)));
typedef __bf16 bf16x2 __attribute__((ext_vector_type(2)));
typedef __amdgpu_buffer_rsrc_t rsrc_t;

#define FK_DELTA 2
#define FK_WIN 30
#define FK_KA 6
#define FK_KB 8
#define FK_QDELAY 450
#define FK_SL0 0
#define FK_SL1 0
#define FK_SL2 0
#define FK_SL3 0
#define FK_TRIG_E 0
#define FK_TRIG_O 0

constexpr int D = 640, NH = 32, SK = 8192, WK = 2048, FF = 1728;
constexpr int DELTA = FK_DELTA, NLE = 32 + DELTA, NLO = 32 - DELTA, WIN = FK_WIN, KA = FK_KA, KB = FK_KB;
constexpr int AUX_NT = 2, AUX_SC1 = 16;
constexpr int OOB = 0x7ffffff0;
constexpr float SCALE = 0.08838834764831845f, L2E = 1.4426950408889634f;

constexpr int WS_E = 0;
constexpr int WS_QP = 4096;
constexpr int WS_OP = WS_QP + 32 * 4 * 8 * 512;
constexpr int WS_AP = WS_OP + 32 * 8 * 640;
constexpr int WS_H = WS_AP + 4 * 32 * 160 * 16;
constexpr int WS_ACT = WS_H + 4 * 160 * 16;
constexpr int WS_END = WS_ACT + 4 * 432 * 16;
constexpr int WS_DIAG = 8 << 20;

constexpr int L_FLAG = 0;
constexpr int L_XN = 64;
constexpr int L_QF = L_XN + 640;
constexpr int L_ML = L_QF + 256;
constexpr int L_OW = L_ML + 64;
constexpr int L_OB = L_OW + 4096;
constexpr int L_RED = L_OB + 1024;
constexpr int L_ST = L_RED + 5120;
constexpr int L_WQ = L_ST + 256;
constexpr int L_IMG = L_WQ + 20480;
constexpr int L_END = L_IMG + 81920;
static_assert(L_XN % 16 == 0 && L_QF % 16 == 0 && L_ML % 16 == 0 && L_OW % 16 == 0 && L_OB % 16 == 0 && L_RED % 16 == 0 && L_ST % 16 == 0 &&
              L_WQ % 16 == 0 && L_IMG % 16 == 0, "LDS alignment");
static_assert(L_END <= 160 * 1024 && L_END > 80 * 1024, "one workgroup per CU");
enum { FL_QA = 0, FL_Q = 1, FL_V = 2, FL_DMA = 3, FL_PART = 4, FL_PUB = 5, FL_M = 6 };

struct Params {
  const char* x; const char* k; const char* v; const char* wq; const char* wo; const char* w1; const char* w3; const char* w2;
  char* out; char* ws;
};

struct Tags { unsigned f0, f1, f2, f3; };

__device__ __forceinline__ rsrc_t mkrs(const void* p, unsigned bytes) { return __builtin_amdgcn_make_buffer_rsrc((void*)p, 0, bytes, 0x00020000); }
template <int AUX> __device__ __forceinline__ v4u bl128(rsrc_t r, int voff, int soff) { return __builtin_amdgcn_raw_buffer_load_b128(r, voff, soff, AUX); }
__device__ __forceinline__ float u2f(unsigned u) { return __builtin_bit_cast(float, u); }
__device__ __forceinline__ unsigned f2u(float f) { return __builtin_bit_cast(unsigned, f); }
__device__ __forceinline__ unsigned pkbf(float lo, float hi) { bf16x2 p = {(__bf16)lo, (__bf16)hi}; return __builtin_bit_cast(unsigned, p); }
__device__ __forceinline__ float blo(unsigned p) { return u2f(p << 16); }
__device__ __forceinline__ float bhi(unsigned p) { return u2f(p & 0xffff0000u); }
__device__ __forceinline__ float rbf(float x) { return blo(pkbf(x, 0.f) & 0xffffu); }
__device__ __forceinline__ float dot2(unsigned a, unsigned b, float c) {
  return __builtin_amdgcn_fdot2_f32_bf16(__builtin_bit_cast(bf16x2, a), __builtin_bit_cast(bf16x2, b), c, false);
}
template <int CTRL> __device__ __forceinline__ float dpp(float x) {
  return u2f((unsigned)__builtin_amdgcn_mov_dpp((int)f2u(x), CTRL, 0xf, 0xf, true));
}
__device__ __forceinline__ float row16_sum(float x) {
  x += dpp<0x128>(x); x += dpp<0x124>(x); x += dpp<0x122>(x); x += dpp<0x121>(x); return x;
}
__device__ __forceinline__ float x16_sum(float x) {
  auto s = __builtin_amdgcn_permlane16_swap(f2u(x), f2u(x), false, false); return u2f(s[0]) + u2f(s[1]);
}
__device__ __forceinline__ float x32_sum(float x) {
  auto s = __builtin_amdgcn_permlane32_swap(f2u(x), f2u(x), false, false); return u2f(s[0]) + u2f(s[1]);
}
__device__ __forceinline__ float x16_max(float x) { auto s = __builtin_amdgcn_permlane16_swap(f2u(x), f2u(x), false, false); return fmaxf(u2f(s[0]), u2f(s[1])); }
__device__ __forceinline__ float x32_max(float x) { auto s = __builtin_amdgcn_permlane32_swap(f2u(x), f2u(x), false, false); return fmaxf(u2f(s[0]), u2f(s[1])); }
__device__ __forceinline__ float wave_sum(float x) { return x32_sum(x16_sum(row16_sum(x))); }
__device__ __forceinline__ f32x4 mfma32(v4u a, v4u b, f32x4 c) {
  return __builtin_amdgcn_mfma_f32_16x16x32_bf16(__builtin_bit_cast(bf16x8, a), __builtin_bit_cast(bf16x8, b), c, 0, 0, 0);
}
__device__ __forceinline__ s16x4 trd(LAS char* L, int off) { return __builtin_amdgcn_ds_read_tr16_b64_v4i16((LAS s16x4*)(L + off)); }
__device__ __forceinline__ v4u trfrag(LAS char* L, int off, int step) {
  const v2u a = __builtin_bit_cast(v2u, trd(L, off)), b = __builtin_bit_cast(v2u, trd(L, off + step));
  return v4u{a.x, a.y, b.x, b.y};
}
__device__ __forceinline__ v4u enc4(float a, float b, float c, float d, const Tags& t) {
  return v4u{(f2u(a) & 0xfffff000u) | t.f0, (f2u(b) & 0xfffff000u) | t.f1, (f2u(c) & 0xfffff000u) | t.f2, (f2u(d) & 0xfffff000u) | t.f3};
}
__device__ __forceinline__ bool tagok(v4u c, const Tags& t) { return (((c.x ^ t.f0) | (c.y ^ t.f1) | (c.z ^ t.f2) | (c.w ^ t.f3)) & 0xfffu) == 0u; }
__device__ __forceinline__ float dec(unsigned w) { return u2f(w & 0xfffff000u); }
__device__ __forceinline__ void publish(rsrc_t rw, int off, v4u c) { __builtin_amdgcn_raw_buffer_store_b128(c, rw, off, 0, AUX_SC1); }
template <int N, int SLEEP = 0> __device__ __forceinline__ void gather(rsrc_t rw, const int (&off)[N], v4u (&c)[N], const Tags& t, unsigned long long deadline) {
  int o[N];
#pragma unroll
  for (int i = 0; i < N; ++i) { o[i] = off[i]; c[i] = v4u{0u, 0u, 0u, 0u}; }
  for (unsigned spins = 1;; ++spins) {
    asm volatile("" ::: "memory");
    v4u tmp[N];
#pragma unroll
    for (int i = 0; i < N; ++i) tmp[i] = bl128<AUX_SC1>(rw, o[i], 0);
    bool ok = true;
#pragma unroll
    for (int i = 0; i < N; ++i) {
      const bool hit = o[i] != OOB && tagok(tmp[i], t);
      if (hit) { c[i] = tmp[i]; o[i] = OOB; }
      ok &= o[i] == OOB;
    }
    if (__all(ok)) break;
    if ((spins & 63u) == 0u && __builtin_amdgcn_s_memrealtime() > deadline) break;
    if (SLEEP) __builtin_amdgcn_s_sleep(SLEEP);
  }
  asm volatile("" ::: "memory");
}
__device__ __forceinline__ void lds_wait_ge(LAS char* L, int word, unsigned want, unsigned long long deadline) {
  volatile LAS unsigned* p = (volatile LAS unsigned*)(L + L_FLAG) + word;
  for (unsigned spins = 1; (unsigned)__builtin_amdgcn_readfirstlane(*p) < want; ++spins) { __builtin_amdgcn_s_sleep(1); if ((spins & 1023u) == 0u && __builtin_amdgcn_s_memrealtime() > deadline) break; }
  asm volatile("" ::: "memory");
}
__device__ __forceinline__ void lds_signal(LAS char* L, int word, int lane) {
  asm volatile("s_waitcnt lgkmcnt(0)" ::: "memory");
  if (lane == 0) (void)__hip_atomic_fetch_add((LAS unsigned*)(L + L_FLAG) + word, 1u, __ATOMIC_RELAXED, __HIP_MEMORY_SCOPE_WORKGROUP);
  asm volatile("" ::: "memory");
}
__device__ __forceinline__ unsigned lds_arrive(LAS char* L, int word, int lane) {
  asm volatile("s_waitcnt lgkmcnt(0)" ::: "memory");
  unsigned old = 0;
  if (lane == 0) old = __hip_atomic_fetch_add((LAS unsigned*)(L + L_FLAG) + word, 1u, __ATOMIC_RELAXED, __HIP_MEMORY_SCOPE_WORKGROUP);
  old = __builtin_amdgcn_readfirstlane(old);
  asm volatile("" ::: "memory");
  return old;
}
__device__ __forceinline__ unsigned pack2(unsigned lo, unsigned hi) { return (lo >> 16) | (hi & 0xffff0000u); }
template <int N> __device__ __forceinline__ unsigned shl_row(unsigned x) { return (unsigned)__builtin_amdgcn_mov_dpp((int)x, 0x100 + N, 0xf, 0xf, true); }
__device__ __forceinline__ void wg_barrier() {
  asm volatile("s_waitcnt lgkmcnt(0)" ::: "memory");
  __builtin_amdgcn_s_barrier();
  asm volatile("" ::: "memory");
}
__device__ __forceinline__ void dma16(const char* gsrc, unsigned lds_dst) {
  unsigned keep;
  asm volatile("s_mov_b32 %0, m0\n\ts_mov_b32 m0, %2\n\ts_nop 0\n\tglobal_load_lds_dwordx4 %1, off nt\n\ts_mov_b32 m0, %0"
               : "=&s"(keep) : "v"(gsrc), "s"(lds_dst) : "memory");
}
#define VM_DRAIN() asm volatile("s_waitcnt vmcnt(0)" ::: "memory")

#define STAMP(i) do { } while (0)
#define STAMP_ARG
#define STAMP_PASS

template <int NL> __device__ __forceinline__ int piece_off(int wbase, int rot, int i) {
  int r = i + rot; r = r >= NL ? r - NL : r;
  return wbase + r * 4096;
}

__device__ __forceinline__ float wg_scale(LAS char* L, float m, float& M, unsigned long long deadline) {
  lds_wait_ge(L, FL_M, 8u, deadline);
  const f32x4 a = *(LAS f32x4*)(L + L_ML), c = *(LAS f32x4*)(L + L_ML + 16);
  M = fmaxf(fmaxf(fmaxf(a[0], a[1]), fmaxf(a[2], a[3])), fmaxf(fmaxf(c[0], c[1]), fmaxf(c[2], c[3])));
  return __builtin_amdgcn_exp2f((m - M) * L2E);
}

template <int NL, int TRIG>
__device__ __forceinline__ void attend(rsrc_t rk, rsrc_t rv, int voff, int wbase, int rot, v4u (&a)[WIN], v4u q, LAS char* L, int w, int lane,
                                       float& M, unsigned long long deadline STAMP_ARG) {
  float s[NL];
#pragma unroll
  for (int i = 0; i < NL; ++i) {
    const v4u kk = a[i % WIN];
    float d0 = dot2(kk.x, q.x, 0.f), d1 = dot2(kk.y, q.y, 0.f);
    d0 = dot2(kk.z, q.z, d0); d1 = dot2(kk.w, q.w, d1);
    s[i] = row16_sum(d0 + d1);
    asm volatile("" : "+v"(s[i]));
    const int n = i + WIN;
    if (n < NL) a[i % WIN] = bl128<AUX_NT>(rk, voff, piece_off<NL>(wbase, rot, n));
    else a[i % WIN] = bl128<AUX_NT>(rv, voff, piece_off<NL>(wbase, rot, n - NL));
    if (i == 2 * NL - WIN - 1 - TRIG) { if (w >= 4) lds_signal(L, FL_V, lane); }
    __builtin_amdgcn_sched_barrier(0);
  }
  STAMP(5);
  float m = -INFINITY;
#pragma unroll
  for (int i = 0; i < NL; i += 2) {
    const unsigned p1 = pkbf(s[i], s[i + 1]);
    const unsigned p2 = pkbf(blo(p1) * SCALE, bhi(p1) * SCALE);
    s[i] = blo(p2); s[i + 1] = bhi(p2);
    m = fmaxf(m, fmaxf(s[i], s[i + 1]));
  }
  m = x32_max(x16_max(m));
  if (lane == 0) *(LAS float*)(L + L_ML + w * 4) = m;
  lds_signal(L, FL_M, lane);
  const float mb = -m * L2E;
  float l = 0.f;
#pragma unroll
  for (int i = 0; i < NL; ++i) { s[i] = __builtin_amdgcn_exp2f(fmaf(s[i], L2E, mb)); l += s[i]; }
  l = x32_sum(x16_sum(l));
  float acc[8], e = 0.f;
#pragma unroll
  for (int k = 0; k < 8; ++k) acc[k] = 0.f;
  __builtin_amdgcn_sched_barrier(0);
#pragma unroll
  for (int i = 0; i < NL; ++i) {
    const v4u vv = a[(NL + i) % WIN];
    const float p = s[i];
    acc[0] = fmaf(p, blo(vv.x), acc[0]); acc[1] = fmaf(p, bhi(vv.x), acc[1]);
    acc[2] = fmaf(p, blo(vv.y), acc[2]); acc[3] = fmaf(p, bhi(vv.y), acc[3]);
    acc[4] = fmaf(p, blo(vv.z), acc[4]); acc[5] = fmaf(p, bhi(vv.z), acc[5]);
    acc[6] = fmaf(p, blo(vv.w), acc[6]); acc[7] = fmaf(p, bhi(vv.w), acc[7]);
    asm volatile("" : "+v"(acc[0]), "+v"(acc[1]), "+v"(acc[2]), "+v"(acc[3]), "+v"(acc[4]), "+v"(acc[5]), "+v"(acc[6]), "+v"(acc[7]));
    const int n = i + WIN;
    if (n < NL) a[(NL + i) % WIN] = bl128<AUX_NT>(rv, voff, piece_off<NL>(wbase, rot, n));
    if (NL + i == 2 * NL - WIN - 1 - TRIG) { if (w >= 4) lds_signal(L, FL_V, lane); }
    if (i == NL / 2) { if (w >= 4) e = wg_scale(L, m, M, deadline); }
    __builtin_amdgcn_sched_barrier(0);
  }
  STAMP(6);
  if (w < 4) e = wg_scale(L, m, M, deadline);
  float r4[4];
#pragma unroll
  for (int k = 0; k < 4; ++k) { auto t = __builtin_amdgcn_permlane32_swap(f2u(acc[k]), f2u(acc[k + 4]), false, false); r4[k] = u2f(t[0]) + u2f(t[1]); }
  auto t0 = __builtin_amdgcn_permlane16_swap(f2u(r4[0]), f2u(r4[1]), false, false);
  auto t1 = __builtin_amdgcn_permlane16_swap(f2u(r4[2]), f2u(r4[3]), false, false);
  const int d = 8 * (lane & 15) + ((lane >> 4) & 1) + 4 * (lane >> 5);
  *(LAS float*)(L + L_OW + w * 512 + d * 4) = e * (u2f(t0[0]) + u2f(t0[1]));
  *(LAS float*)(L + L_OW + w * 512 + d * 4 + 8) = e * (u2f(t1[0]) + u2f(t1[1]));
  if (lane == 0) *(LAS float*)(L + L_ML + 32 + w * 4) = e * l;
}

template <int s>
__device__ __forceinline__ void body(const Params& p, LAS char* L) {
  const unsigned Lb = (unsigned)(uintptr_t)L;
  const int tid = threadIdx.x, lane = tid & 63, w = __builtin_amdgcn_readfirstlane(tid >> 6);
  const int g = blockIdx.x, idx = g >> 1, xp = idx & 3, slot = idx >> 2;
  const int h = xp * 8 + (slot & 7), b = slot >> 3;
  const int lg = lane >> 4, li = lane & 15;
  const unsigned long long t_start = __builtin_amdgcn_s_memrealtime();
  const unsigned long long deadline = t_start + 2000000ull;
  STAMP(0);
  if (tid < 16) ((LAS unsigned*)(L + L_FLAG))[tid] = 0u;
  wg_barrier();
  const rsrc_t rw = mkrs(p.ws, WS_END);
  const int jq = 2 * b + s;
  unsigned xr[5];
  if (w < 4) {
    const rsrc_t rx = mkrs(p.x + w * 1280, 1280);
#pragma unroll
    for (int i = 0; i < 5; ++i) xr[i] = __builtin_amdgcn_raw_buffer_load_b32(rx, lane * 4 + i * 256, 0, 0);
    __builtin_amdgcn_sched_barrier(0);
#pragma unroll
    for (int i = 0; i < 5; ++i) {
      const int n = w * 5 + i;
      dma16(p.wq + (size_t)(80 * jq + 4 * n + lg) * 8192 + h * 256 + li * 16, Lb + L_WQ + n * 1024);
    }
  }
  const unsigned ev = __builtin_amdgcn_raw_buffer_load_b32(rw, WS_E, 0, AUX_SC1);
  __builtin_amdgcn_sched_barrier(0);
  constexpr int nl = s ? NLO : NLE;
  const size_t kvbase = ((size_t)(b * NH + h) * SK + (SK - WK) + (s ? 32 * NLE : 0)) * 256;
  const rsrc_t rk = mkrs(p.k + kvbase, (unsigned)nl * 8192), rv = mkrs(p.v + kvbase, (unsigned)nl * 8192);
  const int voff = lane * 16, wbase = ((w >> 2) * 4 * nl + (w & 3)) * 1024, rot = idx & 15;
  v4u a[WIN];
#pragma unroll
  for (int i = 0; i < KA; ++i) { a[i] = bl128<AUX_NT>(rk, voff, piece_off<nl>(wbase, rot, i)); __builtin_amdgcn_sched_barrier(0); }
  STAMP(1);
  Tags tg;
  if (w < 4) {
    STAMP(12);
    float xv[10]; float sum = 0.f;
#pragma unroll
    for (int i = 0; i < 5; ++i) { xv[2 * i] = blo(xr[i]); xv[2 * i + 1] = bhi(xr[i]); sum += xv[2 * i] + xv[2 * i + 1]; }
    const float mean = wave_sum(sum) * (1.0f / D);
    float sq = 0.f;
#pragma unroll
    for (int i = 0; i < 10; ++i) { const float dd = xv[i] - mean; sq = fmaf(dd, dd, sq); }
    const float var = rbf(wave_sum(sq) * (1.0f / D)), meanb = rbf(mean);
    const float rden = __builtin_amdgcn_rcpf(rbf(__builtin_amdgcn_sqrtf(rbf(var + 1e-5f))));
#pragma unroll
    for (int i = 0; i < 5; ++i) {
      const int id = lane + 64 * i - 40 * jq;
      const unsigned d2 = pkbf(xv[2 * i] - meanb, xv[2 * i + 1] - meanb);
      const unsigned pk = pkbf(blo(d2) * rden, bhi(d2) * rden);
      if (id >= 0 && id < 40) *(LAS unsigned*)(L + L_XN + w * 160 + id * 4) = pk;
    }
  }
  STAMP(3);
  {
    const unsigned u = __builtin_amdgcn_readfirstlane(ev) + 1u;
    tg.f0 = u & 0xfffu; tg.f1 = (u >> 12) & 0xfffu; tg.f2 = (u >> 24) | 0xa00u; tg.f3 = (~u) & 0xfffu;
    asm volatile("" : "+s"(tg.f0), "+s"(tg.f1), "+s"(tg.f2), "+s"(tg.f3) :: "memory");
  }
  STAMP(13);
  if (w < 4) {
    lds_signal(L, FL_QA, lane);
    lds_wait_ge(L, FL_QA, 4u, deadline);
    STAMP(14);
#pragma unroll
    for (int tt = 0; tt < 2; ++tt) {
      const int t = 2 * w + tt;
      f32x4 c = {0.f, 0.f, 0.f, 0.f};
#pragma unroll
      for (int ks = 0; ks < 5; ++ks) {
        const s16x4 af = trd(L, L_WQ + (16 * ks + 4 * lg + (li >> 2)) * 256 + (16 * t + 4 * (li & 3)) * 2);
        const s16x4 bf = *(LAS s16x4*)(L + L_XN + (li & 3) * 160 + (16 * ks + 4 * lg) * 2);
        c = __builtin_amdgcn_mfma_f32_16x16x16bf16_1k(af, bf, c, 0, 0, 0);
      }
      publish(rw, li < 4 ? WS_QP + (((h * 4 + li) * 8 + jq) * 32 + 4 * t + lg) * 16 : OOB, enc4(c[0], c[1], c[2], c[3], tg));
    }
  }
  STAMP(2);
#pragma unroll
  for (int i = KA; i < KB; ++i) { a[i] = bl128<AUX_NT>(rk, voff, piece_off<nl>(wbase, rot, i)); __builtin_amdgcn_sched_barrier(0); }
  if (w == 0) {
    int off[4]; v4u c[4];
#pragma unroll
    for (int i = 0; i < 4; ++i) off[i] = WS_QP + ((h * 4 + b) * 256 + 64 * i + lane) * 16;
    if (FK_QDELAY) { while (__builtin_amdgcn_s_memrealtime() < t_start + FK_QDELAY) __builtin_amdgcn_s_sleep(2); }
    STAMP(15);
    gather<4>(rw, off, c, tg, deadline);
    STAMP(16);
    float q0 = (dec(c[0].x) + dec(c[1].x)) + (dec(c[2].x) + dec(c[3].x)), q1 = (dec(c[0].y) + dec(c[1].y)) + (dec(c[2].y) + dec(c[3].y));
    float q2 = (dec(c[0].z) + dec(c[1].z)) + (dec(c[2].z) + dec(c[3].z)), q3 = (dec(c[0].w) + dec(c[1].w)) + (dec(c[2].w) + dec(c[3].w));
    q0 = x32_sum(q0); q1 = x32_sum(q1); q2 = x32_sum(q2); q3 = x32_sum(q3);
    if (lane < 32) *(LAS v2u*)(L + L_QF + lane * 8) = v2u{pkbf(q0, q1), pkbf(q2, q3)};
    lds_signal(L, FL_Q, lane);
  }
#pragma unroll
  for (int i = KB; i < WIN; ++i) { a[i] = bl128<AUX_NT>(rk, voff, piece_off<nl>(wbase, rot, i)); __builtin_amdgcn_sched_barrier(0); }
  lds_wait_ge(L, FL_Q, 1u, deadline);
  const v4u q = *(LAS v4u*)(L + L_QF + li * 16);
  STAMP(4);
  float M;
  attend<nl, s ? FK_TRIG_O : FK_TRIG_E>(rk, rv, voff, wbase, rot, a, q, L, w, lane, M, deadline STAMP_PASS);
  if (lds_arrive(L, FL_PART, lane) == 7u) {
    LAS char* ow = L + L_OW + (lane >> 5) * 2048 + (lane & 31) * 16;
    const f32x4 oa = *(LAS f32x4*)(ow), ob = *(LAS f32x4*)(ow + 512), oc = *(LAS f32x4*)(ow + 1024), od = *(LAS f32x4*)(ow + 1536);
    const f32x4 la = *(LAS f32x4*)(L + L_ML + 32), lb = *(LAS f32x4*)(L + L_ML + 48);
    float o[4];
#pragma unroll
    for (int k = 0; k < 4; ++k) o[k] = x32_sum((oa[k] + ob[k]) + (oc[k] + od[k]));
    const float lsum = ((la[0] + la[1]) + (la[2] + la[3])) + ((lb[0] + lb[1]) + (lb[2] + lb[3]));
    const int base = WS_OP + (h * 8 + jq) * 640;
    publish(rw, lane < 32 ? base + lane * 16 : (lane == 32 ? base + 512 : OOB),
            lane < 32 ? enc4(o[0], o[1], o[2], o[3], tg) : enc4(M, lsum, 0.f, 0.f, tg));
    lds_signal(L, FL_PUB, lane);
  }
  STAMP(8);
  int role = 0, strip = 0;
  if constexpr (s) role = 1;
  else if (slot < 14) { const int mm = xp + 4 * (slot >> 1); if (mm < 27) { role = 3; strip = 2 * mm + (slot & 1); } }
  else if (slot < 26) { const int sp = slot - 14, n = xp + 4 * (sp >> 2); if (n < 10) { role = 4; strip = 4 * n + (sp & 3); } }
  if (w < 4 && role != 0) {
    lds_wait_ge(L, s ? FL_V : FL_PUB, s ? 4u : 1u, deadline);
    if constexpr (s) {
#pragma unroll
      for (int i = 0; i < 10; ++i) {
        const int n = w * 10 + i, id = n * 64 + lane, r = id / 20, c = id - r * 20;
        dma16(p.wo + (size_t)(128 * h + r) * 1280 + b * 320 + c * 16, Lb + L_IMG + n * 1024);
      }
    } else if (role == 3) {
#pragma unroll
      for (int i = 0; i < 20; ++i) {
        const int n = w * 20 + i, r = n * 8 + (lane >> 3), c = lane & 7;
        dma16(((c & 4) ? p.w3 : p.w1) + (size_t)r * 3456 + strip * 64 + (c & 3) * 16, Lb + L_IMG + n * 1024);
      }
    } else {
#pragma unroll
      for (int i = 0; i < 14; ++i) {
        const int n = w + 4 * i, r = n * 32 + (lane >> 1);
        if (n < 54) dma16(p.w2 + (size_t)r * 1280 + strip * 32 + (lane & 1) * 16, Lb + L_IMG + n * 1024);
      }
    }
    VM_DRAIN();
  }
  STAMP(7);
  const int gl = (li >> 3) * 128 + (((li >> 2) & 1) * 4 + lg) * 16;
  if constexpr (s) {
    v4u af[12];
    int nt = 0, t0 = 0, apo[3];
    if (w < 4) {
      lds_signal(L, FL_DMA, lane);
      lds_wait_ge(L, FL_DMA, 4u, deadline);
      nt = w < 2 ? 3 : 2; t0 = w < 2 ? 3 * w : 2 * w + 2;
#pragma unroll
      for (int tt = 0; tt < 3; ++tt) {
        const int t = (tt < nt) ? t0 + tt : t0;
#pragma unroll
        for (int ks = 0; ks < 4; ++ks) af[tt * 4 + ks] = trfrag(L, L_IMG + (32 * ks + 8 * lg + (li >> 2)) * 320 + (16 * t + 4 * (li & 3)) * 2, 4 * 320);
        const int lc = 16 * t + 4 * lg, u = 8 * b + lc / 20, cc = (lc % 20) >> 2;
        apo[tt] = (li < 4 && tt < nt) ? WS_AP + (((li * 32 + u) * 5 + cc) * 32 + h) * 16 : OOB;
      }
    } else {
      const int bb = w - 4, half = lane >> 5;
      int off[2]; v4u c[2];
      off[0] = WS_OP + (h * 8 + 2 * bb + half) * 640 + (lane & 31) * 16;
      off[1] = WS_OP + (h * 8 + 2 * bb + half) * 640 + 512;
      gather<2, FK_SL0>(rw, off, c, tg, deadline);
      const float ms = dec(c[1].x), ls = dec(c[1].y);
      const float Mx = x32_max(ms), e = __builtin_amdgcn_exp2f((ms - Mx) * L2E), wgt = e * __builtin_amdgcn_rcpf(x32_sum(e * ls));
      const float o0 = x32_sum(wgt * dec(c[0].x)), o1 = x32_sum(wgt * dec(c[0].y)), o2 = x32_sum(wgt * dec(c[0].z)), o3 = x32_sum(wgt * dec(c[0].w));
      if (lane < 32) *(LAS v2u*)(L + L_OB + bb * 256 + lane * 8) = v2u{pkbf(o0, o1), pkbf(o2, o3)};
    }
    STAMP(9);
    wg_barrier();
    if (w < 4) {
      STAMP(17);
      v4u bfr[4];
#pragma unroll
      for (int ks = 0; ks < 4; ++ks) bfr[ks] = *(LAS v4u*)(L + L_OB + (li & 3) * 256 + ks * 64 + lg * 16);
      f32x4 c[3];
#pragma unroll
      for (int tt = 0; tt < 3; ++tt) c[tt] = f32x4{0.f, 0.f, 0.f, 0.f};
#pragma unroll
      for (int ks = 0; ks < 4; ++ks)
#pragma unroll
        for (int tt = 0; tt < 3; ++tt) c[tt] = mfma32(af[tt * 4 + ks], bfr[ks], c[tt]);
#pragma unroll
      for (int tt = 0; tt < 3; ++tt) publish(rw, apo[tt], enc4(c[tt][0], c[tt][1], c[tt][2], c[tt][3], tg));
    } else if (w < 7) {
      const int cc = 2 * (w - 4) + (lane >> 5);
      const bool on = cc < 5, mine = on && (lane & 31) == 0;
      const rsrc_t rx = mkrs(p.x, 4 * 1280);
      const v2u xx = __builtin_amdgcn_raw_buffer_load_b64(rx, mine ? b * 1280 + (20 * h + 4 * cc) * 2 : OOB, 0, 0);
      int off[1]; v4u c[1];
      off[0] = on ? WS_AP + (b * 32 + h) * 2560 + cc * 512 + (lane & 31) * 16 : OOB;
      gather<1, FK_SL1>(rw, off, c, tg, deadline);
      STAMP(10);
      const float r0 = rbf(x16_sum(row16_sum(dec(c[0].x)))), r1 = rbf(x16_sum(row16_sum(dec(c[0].y))));
      const float r2 = rbf(x16_sum(row16_sum(dec(c[0].z)))), r3 = rbf(x16_sum(row16_sum(dec(c[0].w))));
      const unsigned h01 = pkbf(blo(xx.x) + r0, bhi(xx.x) + r1), h23 = pkbf(blo(xx.y) + r2, bhi(xx.y) + r3);
      publish(rw, mine ? WS_H + (b * 160 + 5 * h + cc) * 16 : OOB, enc4(blo(h01), bhi(h01), blo(h23), bhi(h23), tg));
    }
    STAMP(11);
  } else if (role == 3) {
    wg_barrier();
    const int pt = w & 1, kq = w >> 1, np = kq < 2 ? 3 : 2;
    v4u ag[6], au[6];
#pragma unroll
    for (int i = 0; i < 6; ++i) {
      const int ks = 2 * ((i >> 1) < np ? kq + 4 * (i >> 1) : kq) + (i & 1);
      const int ro = L_IMG + (32 * ks + 4 * lg + (li >> 2)) * 128 + pt * 32 + (li & 3) * 8;
      ag[i] = trfrag(L, ro, 16 * 128); au[i] = trfrag(L, ro + 64, 16 * 128);
    }
    int off[3]; v4u c[3];
#pragma unroll
    for (int i = 0; i < 3; ++i) off[i] = i < np ? WS_H + (li & 3) * 2560 + (kq + 4 * i) * 256 + gl : OOB;
    STAMP(9);
    gather<3, FK_SL2>(rw, off, c, tg, deadline);
    STAMP(10);
    f32x4 cg = {0.f, 0.f, 0.f, 0.f}, cu = {0.f, 0.f, 0.f, 0.f};
    float s1 = 0.f, s2 = 0.f;
#pragma unroll
    for (int i = 0; i < 3; ++i) {
      if (i < np) {
        const unsigned p0 = pack2(c[i].x, c[i].y), p1 = pack2(c[i].z, c[i].w);
        unsigned p2 = shl_row<4>(p0), p3 = shl_row<4>(p1), p4 = shl_row<8>(p0), p5 = shl_row<8>(p1), p6 = shl_row<12>(p0), p7 = shl_row<12>(p1);
        asm volatile("" : "+v"(p2), "+v"(p3), "+v"(p4), "+v"(p5), "+v"(p6), "+v"(p7));
        const bool one = li >= 4;
        constexpr unsigned ONE2 = 0x3f803f80u;
        const v4u b0 = {one ? ONE2 : p0, one ? ONE2 : p1, one ? ONE2 : p2, one ? ONE2 : p3};
        const v4u b1 = {one ? ONE2 : p4, one ? ONE2 : p5, one ? ONE2 : p6, one ? ONE2 : p7};
        cg = mfma32(ag[2 * i], b0, cg); cu = mfma32(au[2 * i], b0, cu);
        cg = mfma32(ag[2 * i + 1], b1, cg); cu = mfma32(au[2 * i + 1], b1, cu);
        const float v0 = dec(c[i].x), v1 = dec(c[i].y), v2 = dec(c[i].z), v3 = dec(c[i].w);
        s1 += (v0 + v1) + (v2 + v3);
        s2 = fmaf(v0, v0, fmaf(v1, v1, fmaf(v2, v2, fmaf(v3, v3, s2))));
      }
    }
    s1 += dpp<0x124>(s1); s2 += dpp<0x124>(s2); s1 += dpp<0x128>(s1); s2 += dpp<0x128>(s2);
    s1 = x32_sum(x16_sum(s1)); s2 = x32_sum(x16_sum(s2));
    if (li < 5) {
      *(LAS f32x4*)(L + L_RED + (w * 2 + 0) * 320 + (lg * 5 + li) * 16) = cg;
      *(LAS f32x4*)(L + L_RED + (w * 2 + 1) * 320 + (lg * 5 + li) * 16) = cu;
    }
    if (lane < 4 && pt == 0) *(LAS v2u*)(L + L_ST + (kq * 4 + lane) * 8) = v2u{f2u(s1), f2u(s2)};
    STAMP(18);
    wg_barrier();
    STAMP(19);
    if (w == 0) {
      const int p2 = (lane >> 4) & 1, e = lane & 15, eg = e >> 2, eb = e & 3;
      f32x4 gs = {0.f, 0.f, 0.f, 0.f}, us = gs, gc = gs, uc = gs;
      float t1 = 0.f, t2 = 0.f;
#pragma unroll
      for (int k = 0; k < 4; ++k) {
        LAS char* rb_ = L + L_RED + ((k * 2 + p2) * 2) * 320 + eg * 80;
        gs += *(LAS f32x4*)(rb_ + eb * 16); gc += *(LAS f32x4*)(rb_ + 64);
        us += *(LAS f32x4*)(rb_ + 320 + eb * 16); uc += *(LAS f32x4*)(rb_ + 320 + 64);
        const v2u st = *(LAS v2u*)(L + L_ST + (k * 4 + eb) * 8); t1 += u2f(st.x); t2 += u2f(st.y);
      }
      const float mean = t1 * (1.0f / D), var = rbf(fmaxf(fmaf(-mean, mean, t2 * (1.0f / D)), 0.f)), meanb = rbf(mean);
      const float rden = __builtin_amdgcn_rcpf(rbf(__builtin_amdgcn_sqrtf(rbf(var + 1e-5f))));
      float av[4];
#pragma unroll
      for (int k = 0; k < 4; ++k) {
        const float gg = rbf(rden * fmaf(-meanb, gc[k], gs[k])), uu = rbf(rden * fmaf(-meanb, uc[k], us[k]));
        const float si = rbf(gg * __builtin_amdgcn_rcpf(1.0f + __builtin_amdgcn_exp2f(-gg * L2E)));
        av[k] = rbf(si * uu);
      }
      publish(rw, lane < 32 ? WS_ACT + (eb * 432 + 8 * strip + 4 * p2 + eg) * 16 : OOB, enc4(av[0], av[1], av[2], av[3], tg));
    }
    STAMP(11);
  } else if (role == 4) {
    wg_barrier();
    const int np = w < 3 ? 4 : 3;
    v4u af[8];
#pragma unroll
    for (int i = 0; i < 8; ++i) {
      const int ks = 2 * ((i >> 1) < np ? w + 8 * (i >> 1) : w) + (i & 1);
      af[i] = trfrag(L, L_IMG + (32 * ks + 4 * lg + (li >> 2)) * 32 + (li & 3) * 8, 16 * 32);
    }
    v4u hc[1];
    if (w == 0) {
      int off[1]; off[0] = lane < 16 ? WS_H + ((lane & 3) * 160 + 4 * strip + (lane >> 2)) * 16 : OOB;
      gather<1, FK_SL2>(rw, off, hc, tg, deadline);
    }
    int off[4]; v4u c[4];
#pragma unroll
    for (int i = 0; i < 4; ++i) off[i] = i < np ? WS_ACT + (li & 3) * 6912 + (w + 8 * i) * 256 + gl : OOB;
    STAMP(9);
    gather<4, FK_SL3>(rw, off, c, tg, deadline);
    STAMP(10);
    f32x4 cc = {0.f, 0.f, 0.f, 0.f};
#pragma unroll
    for (int i = 0; i < 4; ++i) {
      if (i < np) {
        const unsigned p0 = pack2(c[i].x, c[i].y), p1 = pack2(c[i].z, c[i].w);
        cc = mfma32(af[2 * i], v4u{p0, p1, shl_row<4>(p0), shl_row<4>(p1)}, cc);
        cc = mfma32(af[2 * i + 1], v4u{shl_row<8>(p0), shl_row<8>(p1), shl_row<12>(p0), shl_row<12>(p1)}, cc);
      }
    }
    if (li < 4) *(LAS f32x4*)(L + L_RED + w * 256 + (lg * 4 + li) * 16) = cc;
    STAMP(18);
    wg_barrier();
    STAMP(19);
    if (w == 0) {
      f32x4 sm = {0.f, 0.f, 0.f, 0.f};
#pragma unroll
      for (int k = 0; k < 8; ++k) sm += *(LAS f32x4*)(L + L_RED + k * 256 + (lane & 15) * 16);
      const v2u o = {pkbf(dec(hc[0].x) + rbf(sm[0]), dec(hc[0].y) + rbf(sm[1])), pkbf(dec(hc[0].z) + rbf(sm[2]), dec(hc[0].w) + rbf(sm[3]))};
      const rsrc_t ro = mkrs(p.out, 4 * 1280);
      __builtin_amdgcn_raw_buffer_store_b64(o, ro, lane < 16 ? (lane & 3) * 1280 + (16 * strip + 4 * (lane >> 2)) * 2 : OOB, 0, 0);
      if (strip == 0) __builtin_amdgcn_raw_buffer_store_b32(__builtin_amdgcn_readfirstlane(ev) + 1u, rw, lane == 0 ? WS_E : OOB, 0, AUX_SC1);
    }
    STAMP(11);
  }
}

__global__ void __launch_bounds__(512, 2) fused_block(Params p) {
  extern __shared__ __attribute__((aligned(16))) char smem[];
  if (blockIdx.x & 1) body<1>(p, (LAS char*)smem);
  else body<0>(p, (LAS char*)smem);
}
}

extern "C" void kernel_launch(void* const* d_in, const int* in_sizes, int n_in, void* d_out, int out_size, void* d_ws, size_t ws_size,
                              hipStream_t stream) {
  (void)in_sizes; (void)n_in; (void)out_size; (void)ws_size;
  fk::Params p;
  p.x = (const char*)d_in[0]; p.k = (const char*)d_in[1]; p.v = (const char*)d_in[2]; p.wq = (const char*)d_in[3]; p.wo = (const char*)d_in[4];
  p.w1 = (const char*)d_in[6]; p.w3 = (const char*)d_in[7]; p.w2 = (const char*)d_in[8];
  p.out = (char*)d_out; p.ws = (char*)d_ws;
  (void)hipFuncSetAttribute((const void*)fk::fused_block, hipFuncAttributeMaxDynamicSharedMemorySize, fk::L_END);
  fk::fused_block<<<256, 512, fk::L_END, stream>>>(p);
}
```

```cpp
#define FK_DIAG
#define FK_PRINT
#include <cstdio>
#include <hip/hip_runtime.h>
#include <hip/hip_bf16.h>
#include <stdint.h>

namespace fk {
#define GAS __attribute__((address_space(1)))
#define LAS __attribute__((address_space(3)))
typedef unsigned v4u __attribute__((ext_vector_type(4)));
typedef unsigned v2u __attribute__((ext_vector_type(2)));
typedef float f32x4 __attribute__((ext_vector_type(4)));
typedef short s16x4 __attribute__((ext_vector_type(4)));
typedef __bf16 bf16x8 __attribute__((ext_vector_type(8)));
typedef __bf16 bf16x2 __attribute__((ext_vector_type(2)));
typedef __amdgpu_buffer_rsrc_t rsrc_t;

#define FK_DELTA 2
#define FK_WIN 30
#define FK_KA 6
#define FK_KB 8
#define FK_QDELAY 450
#define FK_SL0 0
#define FK_SL1 0
#define FK_SL2 0
#define FK_SL3 0
#define FK_TRIG_E 0
#define FK_TRIG_O 0

constexpr int D = 640, NH = 32, SK = 8192, WK = 2048, FF = 1728;
constexpr int DELTA = FK_DELTA, NLE = 32 + DELTA, NLO = 32 - DELTA, WIN = FK_WIN, KA = FK_KA, KB = FK_KB;
constexpr int AUX_NT = 2, AUX_SC1 = 16;
constexpr int OOB = 0x7ffffff0;
constexpr float SCALE = 0.08838834764831845f, L2E = 1.4426950408889634f;

constexpr int WS_E = 0;
constexpr int WS_QP = 4096;
constexpr int WS_OP = WS_QP + 32 * 4 * 8 * 512;
constexpr int WS_AP = WS_OP + 32 * 8 * 640;
constexpr int WS_H = WS_AP + 4 * 32 * 160 * 16;
constexpr int WS_ACT = WS_H + 4 * 160 * 16;
constexpr int WS_END = WS_ACT + 4 * 432 * 16;
constexpr int WS_DIAG = 8 << 20;

constexpr int L_FLAG = 0;
constexpr int L_XN = 64;
constexpr int L_QF = L_XN + 640;
constexpr int L_ML = L_QF + 256;
constexpr int L_OW = L_ML + 64;
constexpr int L_OB = L_OW + 4096;
constexpr int L_RED = L_OB + 1024;
constexpr int L_ST = L_RED + 5120;
constexpr int L_WQ = L_ST + 256;
constexpr int L_IMG = L_WQ + 20480;
constexpr int L_END = L_IMG + 81920;
static_assert(L_XN % 16 == 0 && L_QF % 16 == 0 && L_ML % 16 == 0 && L_OW % 16 == 0 && L_OB % 16 == 0 && L_RED % 16 == 0 && L_ST % 16 == 0 &&
              L_WQ % 16 == 0 && L_IMG % 16 == 0, "LDS alignment");
static_assert(L_END <= 160 * 1024 && L_END > 80 * 1024, "one workgroup per CU");
enum { FL_QA = 0, FL_Q = 1, FL_V = 2, FL_DMA = 3, FL_PART = 4, FL_PUB = 5, FL_M = 6 };

struct Params {
  const char* x; const char* k; const char* v; const char* wq; const char* wo; const char* w1; const char* w3; const char* w2;
  char* out; char* ws;
};

struct Tags { unsigned f0, f1, f2, f3; };

__device__ __forceinline__ rsrc_t mkrs(const void* p, unsigned bytes) { return __builtin_amdgcn_make_buffer_rsrc((void*)p, 0, bytes, 0x00020000); }
template <int AUX> __device__ __forceinline__ v4u bl128(rsrc_t r, int voff, int soff) { return __builtin_amdgcn_raw_buffer_load_b128(r, voff, soff, AUX); }
__device__ __forceinline__ float u2f(unsigned u) { return __builtin_bit_cast(float, u); }
__device__ __forceinline__ unsigned f2u(float f) { return __builtin_bit_cast(unsigned, f); }
__device__ __forceinline__ unsigned pkbf(float lo, float hi) { bf16x2 p = {(__bf16)lo, (__bf16)hi}; return __builtin_bit_cast(unsigned, p); }
__device__ __forceinline__ float blo(unsigned p) { return u2f(p << 16); }
__device__ __forceinline__ float bhi(unsigned p) { return u2f(p & 0xffff0000u); }
__device__ __forceinline__ float rbf(float x) { return blo(pkbf(x, 0.f) & 0xffffu); }
__device__ __forceinline__ float dot2(unsigned a, unsigned b, float c) {
  return __builtin_amdgcn_fdot2_f32_bf16(__builtin_bit_cast(bf16x2, a), __builtin_bit_cast(bf16x2, b), c, false);
}
template <int CTRL> __device__ __forceinline__ float dpp(float x) {
  return u2f((unsigned)__builtin_amdgcn_mov_dpp((int)f2u(x), CTRL, 0xf, 0xf, true));
}
__device__ __forceinline__ float row16_sum(float x) {
  x += dpp<0x128>(x); x += dpp<0x124>(x); x += dpp<0x122>(x); x += dpp<0x121>(x); return x;
}
__device__ __forceinline__ float x16_sum(float x) {
  auto s = __builtin_amdgcn_permlane16_swap(f2u(x), f2u(x), false, false); return u2f(s[0]) + u2f(s[1]);
}
__device__ __forceinline__ float x32_sum(float x) {
  auto s = __builtin_amdgcn_permlane32_swap(f2u(x), f2u(x), false, false); return u2f(s[0]) + u2f(s[1]);
}
__device__ __forceinline__ float x16_max(float x) { auto s = __builtin_amdgcn_permlane16_swap(f2u(x), f2u(x), false, false); return fmaxf(u2f(s[0]), u2f(s[1])); }
__device__ __forceinline__ float x32_max(float x) { auto s = __builtin_amdgcn_permlane32_swap(f2u(x), f2u(x), false, false); return fmaxf(u2f(s[0]), u2f(s[1])); }
__device__ __forceinline__ float wave_sum(float x) { return x32_sum(x16_sum(row16_sum(x))); }
__device__ __forceinline__ f32x4 mfma32(v4u a, v4u b, f32x4 c) {
  return __builtin_amdgcn_mfma_f32_16x16x32_bf16(__builtin_bit_cast(bf16x8, a), __builtin_bit_cast(bf16x8, b), c, 0, 0, 0);
}
__device__ __forceinline__ s16x4 trd(LAS char* L, int off) { return __builtin_amdgcn_ds_read_tr16_b64_v4i16((LAS s16x4*)(L + off)); }
__device__ __forceinline__ v4u trfrag(LAS char* L, int off, int step) {
  const v2u a = __builtin_bit_cast(v2u, trd(L, off)), b = __builtin_bit_cast(v2u, trd(L, off + step));
  return v4u{a.x, a.y, b.x, b.y};
}
__device__ __forceinline__ v4u enc4(float a, float b, float c, float d, const Tags& t) {
  return v4u{(f2u(a) & 0xfffff000u) | t.f0, (f2u(b) & 0xfffff000u) | t.f1, (f2u(c) & 0xfffff000u) | t.f2, (f2u(d) & 0xfffff000u) | t.f3};
}
__device__ __forceinline__ bool tagok(v4u c, const Tags& t) { return (((c.x ^ t.f0) | (c.y ^ t.f1) | (c.z ^ t.f2) | (c.w ^ t.f3)) & 0xfffu) == 0u; }
__device__ __forceinline__ float dec(unsigned w) { return u2f(w & 0xfffff000u); }
__device__ __forceinline__ void publish(rsrc_t rw, int off, v4u c) { __builtin_amdgcn_raw_buffer_store_b128(c, rw, off, 0, AUX_SC1); }
template <int N, int SLEEP = 0> __device__ __forceinline__ void gather(rsrc_t rw, const int (&off)[N], v4u (&c)[N], const Tags& t, unsigned long long deadline) {
  int o[N];
#pragma unroll
  for (int i = 0; i < N; ++i) { o[i] = off[i]; c[i] = v4u{0u, 0u, 0u, 0u}; }
  for (unsigned spins = 1;; ++spins) {
    asm volatile("" ::: "memory");
    v4u tmp[N];
#pragma unroll
    for (int i = 0; i < N; ++i) tmp[i] = bl128<AUX_SC1>(rw, o[i], 0);
    bool ok = true;
#pragma unroll
    for (int i = 0; i < N; ++i) {
      const bool hit = o[i] != OOB && tagok(tmp[i], t);
      if (hit) { c[i] = tmp[i]; o[i] = OOB; }
      ok &= o[i] == OOB;
    }
    if (__all(ok)) break;
    if ((spins & 63u) == 0u && __builtin_amdgcn_s_memrealtime() > deadline) break;
    if (SLEEP) __builtin_amdgcn_s_sleep(SLEEP);
  }
  asm volatile("" ::: "memory");
}
__device__ __forceinline__ void lds_wait_ge(LAS char* L, int word, unsigned want, unsigned long long deadline) {
  volatile LAS unsigned* p = (volatile LAS unsigned*)(L + L_FLAG) + word;
  for (unsigned spins = 1; (unsigned)__builtin_amdgcn_readfirstlane(*p) < want; ++spins) { __builtin_amdgcn_s_sleep(1); if ((spins & 1023u) == 0u && __builtin_amdgcn_s_memrealtime() > deadline) break; }
  asm volatile("" ::: "memory");
}
__device__ __forceinline__ void lds_signal(LAS char* L, int word, int lane) {
  asm volatile("s_waitcnt lgkmcnt(0)" ::: "memory");
  if (lane == 0) (void)__hip_atomic_fetch_add((LAS unsigned*)(L + L_FLAG) + word, 1u, __ATOMIC_RELAXED, __HIP_MEMORY_SCOPE_WORKGROUP);
  asm volatile("" ::: "memory");
}
__device__ __forceinline__ unsigned lds_arrive(LAS char* L, int word, int lane) {
  asm volatile("s_waitcnt lgkmcnt(0)" ::: "memory");
  unsigned old = 0;
  if (lane == 0) old = __hip_atomic_fetch_add((LAS unsigned*)(L + L_FLAG) + word, 1u, __ATOMIC_RELAXED, __HIP_MEMORY_SCOPE_WORKGROUP);
  old = __builtin_amdgcn_readfirstlane(old);
  asm volatile("" ::: "memory");
  return old;
}
__device__ __forceinline__ unsigned pack2(unsigned lo, unsigned hi) { return (lo >> 16) | (hi & 0xffff0000u); }
template <int N> __device__ __forceinline__ unsigned shl_row(unsigned x) { return (unsigned)__builtin_amdgcn_mov_dpp((int)x, 0x100 + N, 0xf, 0xf, true); }
__device__ __forceinline__ void wg_barrier() {
  asm volatile("s_waitcnt lgkmcnt(0)" ::: "memory");
  __builtin_amdgcn_s_barrier();
  asm volatile("" ::: "memory");
}
__device__ __forceinline__ void dma16(const char* gsrc, unsigned lds_dst) {
  unsigned keep;
  asm volatile("s_mov_b32 %0, m0\n\ts_mov_b32 m0, %2\n\ts_nop 0\n\tglobal_load_lds_dwordx4 %1, off nt\n\ts_mov_b32 m0, %0"
               : "=&s"(keep) : "v"(gsrc), "s"(lds_dst) : "memory");
}
#define VM_DRAIN() asm volatile("s_waitcnt vmcnt(0)" ::: "memory")

#define STAMP(i) do { __builtin_amdgcn_sched_barrier(0); stampv[i] = __builtin_amdgcn_s_memrealtime(); __builtin_amdgcn_sched_barrier(0); } while (0)
#define STAMP_ARG , unsigned long long (&stampv)[24]
#define STAMP_PASS , stampv

template <int NL> __device__ __forceinline__ int piece_off(int wbase, int rot, int i) {
  int r = i + rot; r = r >= NL ? r - NL : r;
  return wbase + r * 4096;
}

__device__ __forceinline__ float wg_scale(LAS char* L, float m, float& M, unsigned long long deadline) {
  lds_wait_ge(L, FL_M, 8u, deadline);
  const f32x4 a = *(LAS f32x4*)(L + L_ML), c = *(LAS f32x4*)(L + L_ML + 16);
  M = fmaxf(fmaxf(fmaxf(a[0], a[1]), fmaxf(a[2], a[3])), fmaxf(fmaxf(c[0], c[1]), fmaxf(c[2], c[3])));
  return __builtin_amdgcn_exp2f((m - M) * L2E);
}

template <int NL, int TRIG>
__device__ __forceinline__ void attend(rsrc_t rk, rsrc_t rv, int voff, int wbase, int rot, v4u (&a)[WIN], v4u q, LAS char* L, int w, int lane,
                                       float& M, unsigned long long deadline STAMP_ARG) {
  float s[NL];
#pragma unroll
  for (int i = 0; i < NL; ++i) {
    const v4u kk = a[i % WIN];
    float d0 = dot2(kk.x, q.x, 0.f), d1 = dot2(kk.y, q.y, 0.f);
    d0 = dot2(kk.z, q.z, d0); d1 = dot2(kk.w, q.w, d1);
    s[i] = row16_sum(d0 + d1);
    asm volatile("" : "+v"(s[i]));
    const int n = i + WIN;
    if (n < NL) a[i % WIN] = bl128<AUX_NT>(rk, voff, piece_off<NL>(wbase, rot, n));
    else a[i % WIN] = bl128<AUX_NT>(rv, voff, piece_off<NL>(wbase, rot, n - NL));
    if (i == 2 * NL - WIN - 1 - TRIG) { if (w >= 4) lds_signal(L, FL_V, lane); }
    __builtin_amdgcn_sched_barrier(0);
  }
  STAMP(5);
  float m = -INFINITY;
#pragma unroll
  for (int i = 0; i < NL; i += 2) {
    const unsigned p1 = pkbf(s[i], s[i + 1]);
    const unsigned p2 = pkbf(blo(p1) * SCALE, bhi(p1) * SCALE);
    s[i] = blo(p2); s[i + 1] = bhi(p2);
    m = fmaxf(m, fmaxf(s[i], s[i + 1]));
  }
  m = x32_max(x16_max(m));
  if (lane == 0) *(LAS float*)(L + L_ML + w * 4) = m;
  lds_signal(L, FL_M, lane);
  const float mb = -m * L2E;
  float l = 0.f;
#pragma unroll
  for (int i = 0; i < NL; ++i) { s[i] = __builtin_amdgcn_exp2f(fmaf(s[i], L2E, mb)); l += s[i]; }
  l = x32_sum(x16_sum(l));
  float acc[8], e = 0.f;
#pragma unroll
  for (int k = 0; k < 8; ++k) acc[k] = 0.f;
  __builtin_amdgcn_sched_barrier(0);
#pragma unroll
  for (int i = 0; i < NL; ++i) {
    const v4u vv = a[(NL + i) % WIN];
    const float p = s[i];
    acc[0] = fmaf(p, blo(vv.x), acc[0]); acc[1] = fmaf(p, bhi(vv.x), acc[1]);
    acc[2] = fmaf(p, blo(vv.y), acc[2]); acc[3] = fmaf(p, bhi(vv.y), acc[3]);
    acc[4] = fmaf(p, blo(vv.z), acc[4]); acc[5] = fmaf(p, bhi(vv.z), acc[5]);
    acc[6] = fmaf(p, blo(vv.w), acc[6]); acc[7] = fmaf(p, bhi(vv.w), acc[7]);
    asm volatile("" : "+v"(acc[0]), "+v"(acc[1]), "+v"(acc[2]), "+v"(acc[3]), "+v"(acc[4]), "+v"(acc[5]), "+v"(acc[6]), "+v"(acc[7]));
    const int n = i + WIN;
    if (n < NL) a[(NL + i) % WIN] = bl128<AUX_NT>(rv, voff, piece_off<NL>(wbase, rot, n));
    if (NL + i == 2 * NL - WIN - 1 - TRIG) { if (w >= 4) lds_signal(L, FL_V, lane); }
    if (i == NL / 2) { if (w >= 4) e = wg_scale(L, m, M, deadline); }
    __builtin_amdgcn_sched_barrier(0);
  }
  STAMP(6);
  if (w < 4) e = wg_scale(L, m, M, deadline);
  float r4[4];
#pragma unroll
  for (int k = 0; k < 4; ++k) { auto t = __builtin_amdgcn_permlane32_swap(f2u(acc[k]), f2u(acc[k + 4]), false, false); r4[k] = u2f(t[0]) + u2f(t[1]); }
  auto t0 = __builtin_amdgcn_permlane16_swap(f2u(r4[0]), f2u(r4[1]), false, false);
  auto t1 = __builtin_amdgcn_permlane16_swap(f2u(r4[2]), f2u(r4[3]), false, false);
  const int d = 8 * (lane & 15) + ((lane >> 4) & 1) + 4 * (lane >> 5);
  *(LAS float*)(L + L_OW + w * 512 + d * 4) = e * (u2f(t0[0]) + u2f(t0[1]));
  *(LAS float*)(L + L_OW + w * 512 + d * 4 + 8) = e * (u2f(t1[0]) + u2f(t1[1]));
  if (lane == 0) *(LAS float*)(L + L_ML + 32 + w * 4) = e * l;
}

template <int s>
__device__ __forceinline__ void body(const Params& p, LAS char* L) {
  const unsigned Lb = (unsigned)(uintptr_t)L;
  const int tid = threadIdx.x, lane = tid & 63, w = __builtin_amdgcn_readfirstlane(tid >> 6);
  const int g = blockIdx.x, idx = g >> 1, xp = idx & 3, slot = idx >> 2;
  const int h = xp * 8 + (slot & 7), b = slot >> 3;
  const int lg = lane >> 4, li = lane & 15;
  unsigned long long stampv[24];
#pragma unroll
  for (int i = 0; i < 24; ++i) stampv[i] = 0;
  const unsigned long long cyc0 = __builtin_amdgcn_s_memtime();
  const unsigned long long t_start = __builtin_amdgcn_s_memrealtime();
  const unsigned long long deadline = t_start + 2000000ull;
  STAMP(0);
  if (tid < 16) ((LAS unsigned*)(L + L_FLAG))[tid] = 0u;
  wg_barrier();
  const rsrc_t rw = mkrs(p.ws, WS_END);
  const int jq = 2 * b + s;
  unsigned xr[5];
  if (w < 4) {
    const rsrc_t rx = mkrs(p.x + w * 1280, 1280);
#pragma unroll
    for (int i = 0; i < 5; ++i) xr[i] = __builtin_amdgcn_raw_buffer_load_b32(rx, lane * 4 + i * 256, 0, 0);
    __builtin_amdgcn_sched_barrier(0);
#pragma unroll
    for (int i = 0; i < 5; ++i) {
      const int n = w * 5 + i;
      dma16(p.wq + (size_t)(80 * jq + 4 * n + lg) * 8192 + h * 256 + li * 16, Lb + L_WQ + n * 1024);
    }
  }
  const unsigned ev = __builtin_amdgcn_raw_buffer_load_b32(rw, WS_E, 0, AUX_SC1);
  __builtin_amdgcn_sched_barrier(0);
  constexpr int nl = s ? NLO : NLE;
  const size_t kvbase = ((size_t)(b * NH + h) * SK + (SK - WK) + (s ? 32 * NLE : 0)) * 256;
  const rsrc_t rk = mkrs(p.k + kvbase, (unsigned)nl * 8192), rv = mkrs(p.v + kvbase, (unsigned)nl * 8192);
  const int voff = lane * 16, wbase = ((w >> 2) * 4 * nl + (w & 3)) * 1024, rot = idx & 15;
  v4u a[WIN];
#pragma unroll
  for (int i = 0; i < KA; ++i) { a[i] = bl128<AUX_NT>(rk, voff, piece_off<nl>(wbase, rot, i)); __builtin_amdgcn_sched_barrier(0); }
  STAMP(1);
  Tags tg;
  if (w < 4) {
    asm volatile("" : "+v"(xr[0]), "+v"(xr[4]));
    STAMP(12);
    float xv[10]; float sum = 0.f;
#pragma unroll
    for (int i = 0; i < 5; ++i) { xv[2 * i] = blo(xr[i]); xv[2 * i + 1] = bhi(xr[i]); sum += xv[2 * i] + xv[2 * i + 1]; }
    const float mean = wave_sum(sum) * (1.0f / D);
    float sq = 0.f;
#pragma unroll
    for (int i = 0; i < 10; ++i) { const float dd = xv[i] - mean; sq = fmaf(dd, dd, sq); }
    const float var = rbf(wave_sum(sq) * (1.0f / D)), meanb = rbf(mean);
    const float rden = __builtin_amdgcn_rcpf(rbf(__builtin_amdgcn_sqrtf(rbf(var + 1e-5f))));
#pragma unroll
    for (int i = 0; i < 5; ++i) {
      const int id = lane + 64 * i - 40 * jq;
      const unsigned d2 = pkbf(xv[2 * i] - meanb, xv[2 * i + 1] - meanb);
      const unsigned pk = pkbf(blo(d2) * rden, bhi(d2) * rden);
      if (id >= 0 && id < 40) *(LAS unsigned*)(L + L_XN + w * 160 + id * 4) = pk;
    }
  }
  STAMP(3);
  {
    const unsigned u = __builtin_amdgcn_readfirstlane(ev) + 1u;
    tg.f0 = u & 0xfffu; tg.f1 = (u >> 12) & 0xfffu; tg.f2 = (u >> 24) | 0xa00u; tg.f3 = (~u) & 0xfffu;
    asm volatile("" : "+s"(tg.f0), "+s"(tg.f1), "+s"(tg.f2), "+s"(tg.f3) :: "memory");
  }
  STAMP(13);
  if (w < 4) {
    lds_signal(L, FL_QA, lane);
    lds_wait_ge(L, FL_QA, 4u, deadline);
    STAMP(14);
#pragma unroll
    for (int tt = 0; tt < 2; ++tt) {
      const int t = 2 * w + tt;
      f32x4 c = {0.f, 0.f, 0.f, 0.f};
#pragma unroll
      for (int ks = 0; ks < 5; ++ks) {
        const s16x4 af = trd(L, L_WQ + (16 * ks + 4 * lg + (li >> 2)) * 256 + (16 * t + 4 * (li & 3)) * 2);
        const s16x4 bf = *(LAS s16x4*)(L + L_XN + (li & 3) * 160 + (16 * ks + 4 * lg) * 2);
        c = __builtin_amdgcn_mfma_f32_16x16x16bf16_1k(af, bf, c, 0, 0, 0);
      }
      publish(rw, li < 4 ? WS_QP + (((h * 4 + li) * 8 + jq) * 32 + 4 * t + lg) * 16 : OOB, enc4(c[0], c[1], c[2], c[3], tg));
    }
  }
  STAMP(2);
#pragma unroll
  for (int i = KA; i < KB; ++i) { a[i] = bl128<AUX_NT>(rk, voff, piece_off<nl>(wbase, rot, i)); __builtin_amdgcn_sched_barrier(0); }
  if (w == 0) {
    int off[4]; v4u c[4];
#pragma unroll
    for (int i = 0; i < 4; ++i) off[i] = WS_QP + ((h * 4 + b) * 256 + 64 * i + lane) * 16;
    if (FK_QDELAY) { while (__builtin_amdgcn_s_memrealtime() < t_start + FK_QDELAY) __builtin_amdgcn_s_sleep(2); }
    STAMP(15);
    gather<4>(rw, off, c, tg, deadline);
    STAMP(16);
    float q0 = (dec(c[0].x) + dec(c[1].x)) + (dec(c[2].x) + dec(c[3].x)), q1 = (dec(c[0].y) + dec(c[1].y)) + (dec(c[2].y) + dec(c[3].y));
    float q2 = (dec(c[0].z) + dec(c[1].z)) + (dec(c[2].z) + dec(c[3].z)), q3 = (dec(c[0].w) + dec(c[1].w)) + (dec(c[2].w) + dec(c[3].w));
    q0 = x32_sum(q0); q1 = x32_sum(q1); q2 = x32_sum(q2); q3 = x32_sum(q3);
    if (lane < 32) *(LAS v2u*)(L + L_QF + lane * 8) = v2u{pkbf(q0, q1), pkbf(q2, q3)};
    lds_signal(L, FL_Q, lane);
  }
#pragma unroll
  for (int i = KB; i < WIN; ++i) { a[i] = bl128<AUX_NT>(rk, voff, piece_off<nl>(wbase, rot, i)); __builtin_amdgcn_sched_barrier(0); }
  lds_wait_ge(L, FL_Q, 1u, deadline);
  const v4u q = *(LAS v4u*)(L + L_QF + li * 16);
  STAMP(4);
  float M;
  attend<nl, s ? FK_TRIG_O : FK_TRIG_E>(rk, rv, voff, wbase, rot, a, q, L, w, lane, M, deadline STAMP_PASS);
  if (lds_arrive(L, FL_PART, lane) == 7u) {
    LAS char* ow = L + L_OW + (lane >> 5) * 2048 + (lane & 31) * 16;
    const f32x4 oa = *(LAS f32x4*)(ow), ob = *(LAS f32x4*)(ow + 512), oc = *(LAS f32x4*)(ow + 1024), od = *(LAS f32x4*)(ow + 1536);
    const f32x4 la = *(LAS f32x4*)(L + L_ML + 32), lb = *(LAS f32x4*)(L + L_ML + 48);
    float o[4];
#pragma unroll
    for (int k = 0; k < 4; ++k) o[k] = x32_sum((oa[k] + ob[k]) + (oc[k] + od[k]));
    const float lsum = ((la[0] + la[1]) + (la[2] + la[3])) + ((lb[0] + lb[1]) + (lb[2] + lb[3]));
    const int base = WS_OP + (h * 8 + jq) * 640;
    publish(rw, lane < 32 ? base + lane * 16 : (lane == 32 ? base + 512 : OOB),
            lane < 32 ? enc4(o[0], o[1], o[2], o[3], tg) : enc4(M, lsum, 0.f, 0.f, tg));
    lds_signal(L, FL_PUB, lane);
  }
  STAMP(8);
  int role = 0, strip = 0;
  if constexpr (s) role = 1;
  else if (slot < 14) { const int mm = xp + 4 * (slot >> 1); if (mm < 27) { role = 3; strip = 2 * mm + (slot & 1); } }
  else if (slot < 26) { const int sp = slot - 14, n = xp + 4 * (sp >> 2); if (n < 10) { role = 4; strip = 4 * n + (sp & 3); } }
  if (w < 4 && role != 0) {
    lds_wait_ge(L, s ? FL_V : FL_PUB, s ? 4u : 1u, deadline);
    if constexpr (s) {
#pragma unroll
      for (int i = 0; i < 10; ++i) {
        const int n = w * 10 + i, id = n * 64 + lane, r = id / 20, c = id - r * 20;
        dma16(p.wo + (size_t)(128 * h + r) * 1280 + b * 320 + c * 16, Lb + L_IMG + n * 1024);
      }
    } else if (role == 3) {
#pragma unroll
      for (int i = 0; i < 20; ++i) {
        const int n = w * 20 + i, r = n * 8 + (lane >> 3), c = lane & 7;
        dma16(((c & 4) ? p.w3 : p.w1) + (size_t)r * 3456 + strip * 64 + (c & 3) * 16, Lb + L_IMG + n * 1024);
      }
    } else {
#pragma unroll
      for (int i = 0; i < 14; ++i) {
        const int n = w + 4 * i, r = n * 32 + (lane >> 1);
        if (n < 54) dma16(p.w2 + (size_t)r * 1280 + strip * 32 + (lane & 1) * 16, Lb + L_IMG + n * 1024);
      }
    }
    VM_DRAIN();
  }
  STAMP(7);
  const int gl = (li >> 3) * 128 + (((li >> 2) & 1) * 4 + lg) * 16;
  if constexpr (s) {
    v4u af[12];
    int nt = 0, t0 = 0, apo[3];
    if (w < 4) {
      lds_signal(L, FL_DMA, lane);
      lds_wait_ge(L, FL_DMA, 4u, deadline);
      nt = w < 2 ? 3 : 2; t0 = w < 2 ? 3 * w : 2 * w + 2;
#pragma unroll
      for (int tt = 0; tt < 3; ++tt) {
        const int t = (tt < nt) ? t0 + tt : t0;
#pragma unroll
        for (int ks = 0; ks < 4; ++ks) af[tt * 4 + ks] = trfrag(L, L_IMG + (32 * ks + 8 * lg + (li >> 2)) * 320 + (16 * t + 4 * (li & 3)) * 2, 4 * 320);
        const int lc = 16 * t + 4 * lg, u = 8 * b + lc / 20, cc = (lc % 20) >> 2;
        apo[tt] = (li < 4 && tt < nt) ? WS_AP + (((li * 32 + u) * 5 + cc) * 32 + h) * 16 : OOB;
      }
    } else {
      const int bb = w - 4, half = lane >> 5;
      int off[2]; v4u c[2];
      off[0] = WS_OP + (h * 8 + 2 * bb + half) * 640 + (lane & 31) * 16;
      off[1] = WS_OP + (h * 8 + 2 * bb + half) * 640 + 512;
      gather<2, FK_SL0>(rw, off, c, tg, deadline);
      const float ms = dec(c[1].x), ls = dec(c[1].y);
      const float Mx = x32_max(ms), e = __builtin_amdgcn_exp2f((ms - Mx) * L2E), wgt = e * __builtin_amdgcn_rcpf(x32_sum(e * ls));
      const float o0 = x32_sum(wgt * dec(c[0].x)), o1 = x32_sum(wgt * dec(c[0].y)), o2 = x32_sum(wgt * dec(c[0].z)), o3 = x32_sum(wgt * dec(c[0].w));
      if (lane < 32) *(LAS v2u*)(L + L_OB + bb * 256 + lane * 8) = v2u{pkbf(o0, o1), pkbf(o2, o3)};
    }
    STAMP(9);
    wg_barrier();
    if (w < 4) {
      STAMP(17);
      v4u bfr[4];
#pragma unroll
      for (int ks = 0; ks < 4; ++ks) bfr[ks] = *(LAS v4u*)(L + L_OB + (li & 3) * 256 + ks * 64 + lg * 16);
      f32x4 c[3];
#pragma unroll
      for (int tt = 0; tt < 3; ++tt) c[tt] = f32x4{0.f, 0.f, 0.f, 0.f};
#pragma unroll
      for (int ks = 0; ks < 4; ++ks)
#pragma unroll
        for (int tt = 0; tt < 3; ++tt) c[tt] = mfma32(af[tt * 4 + ks], bfr[ks], c[tt]);
#pragma unroll
      for (int tt = 0; tt < 3; ++tt) publish(rw, apo[tt], enc4(c[tt][0], c[tt][1], c[tt][2], c[tt][3], tg));
    } else if (w < 7) {
      const int cc = 2 * (w - 4) + (lane >> 5);
      const bool on = cc < 5, mine = on && (lane & 31) == 0;
      const rsrc_t rx = mkrs(p.x, 4 * 1280);
      const v2u xx = __builtin_amdgcn_raw_buffer_load_b64(rx, mine ? b * 1280 + (20 * h + 4 * cc) * 2 : OOB, 0, 0);
      int off[1]; v4u c[1];
      off[0] = on ? WS_AP + (b * 32 + h) * 2560 + cc * 512 + (lane & 31) * 16 : OOB;
      gather<1, FK_SL1>(rw, off, c, tg, deadline);
      STAMP(10);
      const float r0 = rbf(x16_sum(row16_sum(dec(c[0].x)))), r1 = rbf(x16_sum(row16_sum(dec(c[0].y))));
      const float r2 = rbf(x16_sum(row16_sum(dec(c[0].z)))), r3 = rbf(x16_sum(row16_sum(dec(c[0].w))));
      const unsigned h01 = pkbf(blo(xx.x) + r0, bhi(xx.x) + r1), h23 = pkbf(blo(xx.y) + r2, bhi(xx.y) + r3);
      publish(rw, mine ? WS_H + (b * 160 + 5 * h + cc) * 16 : OOB, enc4(blo(h01), bhi(h01), blo(h23), bhi(h23), tg));
    }
    STAMP(11);
  } else if (role == 3) {
    wg_barrier();
    const int pt = w & 1, kq = w >> 1, np = kq < 2 ? 3 : 2;
    v4u ag[6], au[6];
#pragma unroll
    for (int i = 0; i < 6; ++i) {
      const int ks = 2 * ((i >> 1) < np ? kq + 4 * (i >> 1) : kq) + (i & 1);
      const int ro = L_IMG + (32 * ks + 4 * lg + (li >> 2)) * 128 + pt * 32 + (li & 3) * 8;
      ag[i] = trfrag(L, ro, 16 * 128); au[i] = trfrag(L, ro + 64, 16 * 128);
    }
    int off[3]; v4u c[3];
#pragma unroll
    for (int i = 0; i < 3; ++i) off[i] = i < np ? WS_H + (li & 3) * 2560 + (kq + 4 * i) * 256 + gl : OOB;
    STAMP(9);
    gather<3, FK_SL2>(rw, off, c, tg, deadline);
    STAMP(10);
    f32x4 cg = {0.f, 0.f, 0.f, 0.f}, cu = {0.f, 0.f, 0.f, 0.f};
    float s1 = 0.f, s2 = 0.f;
#pragma unroll
    for (int i = 0; i < 3; ++i) {
      if (i < np) {
        const unsigned p0 = pack2(c[i].x, c[i].y), p1 = pack2(c[i].z, c[i].w);
        unsigned p2 = shl_row<4>(p0), p3 = shl_row<4>(p1), p4 = shl_row<8>(p0), p5 = shl_row<8>(p1), p6 = shl_row<12>(p0), p7 = shl_row<12>(p1);
        asm volatile("" : "+v"(p2), "+v"(p3), "+v"(p4), "+v"(p5), "+v"(p6), "+v"(p7));
        const bool one = li >= 4;
        constexpr unsigned ONE2 = 0x3f803f80u;
        const v4u b0 = {one ? ONE2 : p0, one ? ONE2 : p1, one ? ONE2 : p2, one ? ONE2 : p3};
        const v4u b1 = {one ? ONE2 : p4, one ? ONE2 : p5, one ? ONE2 : p6, one ? ONE2 : p7};
        cg = mfma32(ag[2 * i], b0, cg); cu = mfma32(au[2 * i], b0, cu);
        cg = mfma32(ag[2 * i + 1], b1, cg); cu = mfma32(au[2 * i + 1], b1, cu);
        const float v0 = dec(c[i].x), v1 = dec(c[i].y), v2 = dec(c[i].z), v3 = dec(c[i].w);
        s1 += (v0 + v1) + (v2 + v3);
        s2 = fmaf(v0, v0, fmaf(v1, v1, fmaf(v2, v2, fmaf(v3, v3, s2))));
      }
    }
    s1 += dpp<0x124>(s1); s2 += dpp<0x124>(s2); s1 += dpp<0x128>(s1); s2 += dpp<0x128>(s2);
    s1 = x32_sum(x16_sum(s1)); s2 = x32_sum(x16_sum(s2));
    if (li < 5) {
      *(LAS f32x4*)(L + L_RED + (w * 2 + 0) * 320 + (lg * 5 + li) * 16) = cg;
      *(LAS f32x4*)(L + L_RED + (w * 2 + 1) * 320 + (lg * 5 + li) * 16) = cu;
    }
    if (lane < 4 && pt == 0) *(LAS v2u*)(L + L_ST + (kq * 4 + lane) * 8) = v2u{f2u(s1), f2u(s2)};
    STAMP(18);
    wg_barrier();
    STAMP(19);
    if (w == 0) {
      const int p2 = (lane >> 4) & 1, e = lane & 15, eg = e >> 2, eb = e & 3;
      f32x4 gs = {0.f, 0.f, 0.f, 0.f}, us = gs, gc = gs, uc = gs;
      float t1 = 0.f, t2 = 0.f;
#pragma unroll
      for (int k = 0; k < 4; ++k) {
        LAS char* rb_ = L + L_RED + ((k * 2 + p2) * 2) * 320 + eg * 80;
        gs += *(LAS f32x4*)(rb_ + eb * 16); gc += *(LAS f32x4*)(rb_ + 64);
        us += *(LAS f32x4*)(rb_ + 320 + eb * 16); uc += *(LAS f32x4*)(rb_ + 320 + 64);
        const v2u st = *(LAS v2u*)(L + L_ST + (k * 4 + eb) * 8); t1 += u2f(st.x); t2 += u2f(st.y);
      }
      const float mean = t1 * (1.0f / D), var = rbf(fmaxf(fmaf(-mean, mean, t2 * (1.0f / D)), 0.f)), meanb = rbf(mean);
      const float rden = __builtin_amdgcn_rcpf(rbf(__builtin_amdgcn_sqrtf(rbf(var + 1e-5f))));
      float av[4];
#pragma unroll
      for (int k = 0; k < 4; ++k) {
        const float gg = rbf(rden * fmaf(-meanb, gc[k], gs[k])), uu = rbf(rden * fmaf(-meanb, uc[k], us[k]));
        const float si = rbf(gg * __builtin_amdgcn_rcpf(1.0f + __builtin_amdgcn_exp2f(-gg * L2E)));
        av[k] = rbf(si * uu);
      }
      publish(rw, lane < 32 ? WS_ACT + (eb * 432 + 8 * strip + 4 * p2 + eg) * 16 : OOB, enc4(av[0], av[1], av[2], av[3], tg));
    }
    STAMP(11);
  } else if (role == 4) {
    wg_barrier();
    const int np = w < 3 ? 4 : 3;
    v4u af[8];
#pragma unroll
    for (int i = 0; i < 8; ++i) {
      const int ks = 2 * ((i >> 1) < np ? w + 8 * (i >> 1) : w) + (i & 1);
      af[i] = trfrag(L, L_IMG + (32 * ks + 4 * lg + (li >> 2)) * 32 + (li & 3) * 8, 16 * 32);
    }
    v4u hc[1];
    if (w == 0) {
      int off[1]; off[0] = lane < 16 ? WS_H + ((lane & 3) * 160 + 4 * strip + (lane >> 2)) * 16 : OOB;
      gather<1, FK_SL2>(rw, off, hc, tg, deadline);
    }
    int off[4]; v4u c[4];
#pragma unroll
    for (int i = 0; i < 4; ++i) off[i] = i < np ? WS_ACT + (li & 3) * 6912 + (w + 8 * i) * 256 + gl : OOB;
    STAMP(9);
    gather<4, FK_SL3>(rw, off, c, tg, deadline);
    STAMP(10);
    f32x4 cc = {0.f, 0.f, 0.f, 0.f};
#pragma unroll
    for (int i = 0; i < 4; ++i) {
      if (i < np) {
        const unsigned p0 = pack2(c[i].x, c[i].y), p1 = pack2(c[i].z, c[i].w);
        cc = mfma32(af[2 * i], v4u{p0, p1, shl_row<4>(p0), shl_row<4>(p1)}, cc);
        cc = mfma32(af[2 * i + 1], v4u{shl_row<8>(p0), shl_row<8>(p1), shl_row<12>(p0), shl_row<12>(p1)}, cc);
      }
    }
    if (li < 4) *(LAS f32x4*)(L + L_RED + w * 256 + (lg * 4 + li) * 16) = cc;
    STAMP(18);
    wg_barrier();
    STAMP(19);
    if (w == 0) {
      f32x4 sm = {0.f, 0.f, 0.f, 0.f};
#pragma unroll
      for (int k = 0; k < 8; ++k) sm += *(LAS f32x4*)(L + L_RED + k * 256 + (lane & 15) * 16);
      const v2u o = {pkbf(dec(hc[0].x) + rbf(sm[0]), dec(hc[0].y) + rbf(sm[1])), pkbf(dec(hc[0].z) + rbf(sm[2]), dec(hc[0].w) + rbf(sm[3]))};
      const rsrc_t ro = mkrs(p.out, 4 * 1280);
      __builtin_amdgcn_raw_buffer_store_b64(o, ro, lane < 16 ? (lane & 3) * 1280 + (16 * strip + 4 * (lane >> 2)) * 2 : OOB, 0, 0);
      if (strip == 0) __builtin_amdgcn_raw_buffer_store_b32(__builtin_amdgcn_readfirstlane(ev) + 1u, rw, lane == 0 ? WS_E : OOB, 0, AUX_SC1);
    }
    STAMP(11);
  }
  STAMP(23);
  if (lane == 0 && ((role == 4 && strip == 0 && (w == 0 || w == 4)) || (g == 1 && (w == 0 || w == 4)))) {
    const unsigned long long z = stampv[0];
    printf("DG g%d w%d: x%d ln%d pub%d pl%d q%d/%d K%d V%d op%d d7_%d s9_%d s10_%d s11_%d end%d\n", g, w, (int)(stampv[12] - z), (int)(stampv[3] - z), (int)(stampv[2] - z), (int)(stampv[15] - z), (int)(stampv[16] - z), (int)(stampv[4] - z),
           (int)(stampv[5] - z), (int)(stampv[6] - z), (int)(stampv[8] - z), (int)(stampv[7] - z), (int)(stampv[9] - z), (int)(stampv[10] - z), (int)(stampv[11] - z), (int)(stampv[23] - z));
  }
  if (lane == 0) {
    unsigned long long* st = (unsigned long long*)(p.ws + WS_DIAG) + (size_t)(g * 8 + w) * 32;
#pragma unroll
    for (int i = 0; i < 24; ++i) st[i] = stampv[i];
    st[31] = __builtin_amdgcn_s_memtime() - cyc0;
    st[29] = role; st[30] = __builtin_amdgcn_s_getreg((20 << 0) | (0 << 6) | (3 << 11));
  }
}

__global__ void __launch_bounds__(512, 2) fused_block(Params p) {
  extern __shared__ __attribute__((aligned(16))) char smem[];
  if (blockIdx.x & 1) body<1>(p, (LAS char*)smem);
  else body<0>(p, (LAS char*)smem);
}
}

extern "C" void kernel_launch(void* const* d_in, const int* in_sizes, int n_in, void* d_out, int out_size, void* d_ws, size_t ws_size,
                              hipStream_t stream) {
  (void)in_sizes; (void)n_in; (void)out_size; (void)ws_size;
  fk::Params p;
  p.x = (const char*)d_in[0]; p.k = (const char*)d_in[1]; p.v = (const char*)d_in[2]; p.wq = (const char*)d_in[3]; p.wo = (const char*)d_in[4];
  p.w1 = (const char*)d_in[6]; p.w3 = (const char*)d_in[7]; p.w2 = (const char*)d_in[8];
  p.out = (char*)d_out; p.ws = (char*)d_ws;
  (void)hipFuncSetAttribute((const void*)fk::fused_block, hipFuncAttributeMaxDynamicSharedMemorySize, fk::L_END);
  fk::fused_block<<<256, 512, fk::L_END, stream>>>(p);
}
```

```cpp
#define FK_DIAG
#include <hip/hip_runtime.h>
#include <hip/hip_bf16.h>
#include <stdint.h>

namespace fk {
#define GAS __attribute__((address_space(1)))
#define LAS __attribute__((address_space(3)))
typedef unsigned v4u __attribute__((ext_vector_type(4)));
typedef unsigned v2u __attribute__((ext_vector_type(2)));
typedef float f32x4 __attribute__((ext_vector_type(4)));
typedef short s16x4 __attribute__((ext_vector_type(4)));
typedef __bf16 bf16x8 __attribute__((ext_vector_type(8)));
typedef __bf16 bf16x2 __attribute__((ext_vector_type(2)));
typedef __amdgpu_buffer_rsrc_t rsrc_t;

#define FK_DELTA 2
#define FK_WIN 30
#define FK_KA 6
#define FK_KB 8
#define FK_QDELAY 450
#define FK_SL0 0
#define FK_SL1 0
#define FK_SL2 0
#define FK_SL3 0
#define FK_TRIG_E 0
#define FK_TRIG_O 0

constexpr int D = 640, NH = 32, SK = 8192, WK = 2048, FF = 1728;
constexpr int DELTA = FK_DELTA, NLE = 32 + DELTA, NLO = 32 - DELTA, WIN = FK_WIN, KA = FK_KA, KB = FK_KB;
constexpr int AUX_NT = 2, AUX_SC1 = 16;
constexpr int OOB = 0x7ffffff0;
constexpr float SCALE = 0.08838834764831845f, L2E = 1.4426950408889634f;

constexpr int WS_E = 0;
constexpr int WS_QP = 4096;
constexpr int WS_OP = WS_QP + 32 * 4 * 8 * 512;
constexpr int WS_AP = WS_OP + 32 * 8 * 640;
constexpr int WS_H = WS_AP + 4 * 32 * 160 * 16;
constexpr int WS_ACT = WS_H + 4 * 160 * 16;
constexpr int WS_END = WS_ACT + 4 * 432 * 16;
constexpr int WS_DIAG = 8 << 20;

constexpr int L_FLAG = 0;
constexpr int L_XN = 64;
constexpr int L_QF = L_XN + 640;
constexpr int L_ML = L_QF + 256;
constexpr int L_OW = L_ML + 64;
constexpr int L_OB = L_OW + 4096;
constexpr int L_RED = L_OB + 1024;
constexpr int L_ST = L_RED + 5120;
constexpr int L_WQ = L_ST + 256;
constexpr int L_IMG = L_WQ + 20480;
constexpr int L_END = L_IMG + 81920;
static_assert(L_XN % 16 == 0 && L_QF % 16 == 0 && L_ML % 16 == 0 && L_OW % 16 == 0 && L_OB % 16 == 0 && L_RED % 16 == 0 && L_ST % 16 == 0 &&
              L_WQ % 16 == 0 && L_IMG % 16 == 0, "LDS alignment");
static_assert(L_END <= 160 * 1024 && L_END > 80 * 1024, "one workgroup per CU");
enum { FL_QA = 0, FL_Q = 1, FL_V = 2, FL_DMA = 3, FL_PART = 4, FL_PUB = 5, FL_M = 6 };

struct Params {
  const char* x; const char* k; const char* v; const char* wq; const char* wo; const char* w1; const char* w3; const char* w2;
  char* out; char* ws;
};

struct Tags { unsigned f0, f1, f2, f3; };

__device__ __forceinline__ rsrc_t mkrs(const void* p, unsigned bytes) { return __builtin_amdgcn_make_buffer_rsrc((void*)p, 0, bytes, 0x00020000); }
template <int AUX> __device__ __forceinline__ v4u bl128(rsrc_t r, int voff, int soff) { return __builtin_amdgcn_raw_buffer_load_b128(r, voff, soff, AUX); }
__device__ __forceinline__ float u2f(unsigned u) { return __builtin_bit_cast(float, u); }
__device__ __forceinline__ unsigned f2u(float f) { return __builtin_bit_cast(unsigned, f); }
__device__ __forceinline__ unsigned pkbf(float lo, float hi) { bf16x2 p = {(__bf16)lo, (__bf16)hi}; return __builtin_bit_cast(unsigned, p); }
__device__ __forceinline__ float blo(unsigned p) { return u2f(p << 16); }
__device__ __forceinline__ float bhi(unsigned p) { return u2f(p & 0xffff0000u); }
__device__ __forceinline__ float rbf(float x) { return blo(pkbf(x, 0.f) & 0xffffu); }
__device__ __forceinline__ float dot2(unsigned a, unsigned b, float c) {
  return __builtin_amdgcn_fdot2_f32_bf16(__builtin_bit_cast(bf16x2, a), __builtin_bit_cast(bf16x2, b), c, false);
}
template <int CTRL> __device__ __forceinline__ float dpp(float x) {
  return u2f((unsigned)__builtin_amdgcn_mov_dpp((int)f2u(x), CTRL, 0xf, 0xf, true));
}
__device__ __forceinline__ float row16_sum(float x) {
  x += dpp<0x128>(x); x += dpp<0x124>(x); x += dpp<0x122>(x); x += dpp<0x121>(x); return x;
}
__device__ __forceinline__ float x16_sum(float x) {
  auto s = __builtin_amdgcn_permlane16_swap(f2u(x), f2u(x), false, false); return u2f(s[0]) + u2f(s[1]);
}
__device__ __forceinline__ float x32_sum(float x) {
  auto s = __builtin_amdgcn_permlane32_swap(f2u(x), f2u(x), false, false); return u2f(s[0]) + u2f(s[1]);
}
__device__ __forceinline__ float x16_max(float x) { auto s = __builtin_amdgcn_permlane16_swap(f2u(x), f2u(x), false, false); return fmaxf(u2f(s[0]), u2f(s[1])); }
__device__ __forceinline__ float x32_max(float x) { auto s = __builtin_amdgcn_permlane32_swap(f2u(x), f2u(x), false, false); return fmaxf(u2f(s[0]), u2f(s[1])); }
__device__ __forceinline__ float wave_sum(float x) { return x32_sum(x16_sum(row16_sum(x))); }
__device__ __forceinline__ f32x4 mfma32(v4u a, v4u b, f32x4 c) {
  return __builtin_amdgcn_mfma_f32_16x16x32_bf16(__builtin_bit_cast(bf16x8, a), __builtin_bit_cast(bf16x8, b), c, 0, 0, 0);
}
__device__ __forceinline__ s16x4 trd(LAS char* L, int off) { return __builtin_amdgcn_ds_read_tr16_b64_v4i16((LAS s16x4*)(L + off)); }
__device__ __forceinline__ v4u trfrag(LAS char* L, int off, int step) {
  const v2u a = __builtin_bit_cast(v2u, trd(L, off)), b = __builtin_bit_cast(v2u, trd(L, off + step));
  return v4u{a.x, a.y, b.x, b.y};
}
__device__ __forceinline__ v4u enc4(float a, float b, float c, float d, const Tags& t) {
  return v4u{(f2u(a) & 0xfffff000u) | t.f0, (f2u(b) & 0xfffff000u) | t.f1, (f2u(c) & 0xfffff000u) | t.f2, (f2u(d) & 0xfffff000u) | t.f3};
}
__device__ __forceinline__ bool tagok(v4u c, const Tags& t) { return (((c.x ^ t.f0) | (c.y ^ t.f1) | (c.z ^ t.f2) | (c.w ^ t.f3)) & 0xfffu) == 0u; }
__device__ __forceinline__ float dec(unsigned w) { return u2f(w & 0xfffff000u); }
__device__ __forceinline__ void publish(rsrc_t rw, int off, v4u c) { __builtin_amdgcn_raw_buffer_store_b128(c, rw, off, 0, AUX_SC1); }
template <int N, int SLEEP = 0> __device__ __forceinline__ void gather(rsrc_t rw, const int (&off)[N], v4u (&c)[N], const Tags& t, unsigned long long deadline) {
  int o[N];
#pragma unroll
  for (int i = 0; i < N; ++i) { o[i] = off[i]; c[i] = v4u{0u, 0u, 0u, 0u}; }
  for (unsigned spins = 1;; ++spins) {
    asm volatile("" ::: "memory");
    v4u tmp[N];
#pragma unroll
    for (int i = 0; i < N; ++i) tmp[i] = bl128<AUX_SC1>(rw, o[i], 0);
    bool ok = true;
#pragma unroll
    for (int i = 0; i < N; ++i) {
      const bool hit = o[i] != OOB && tagok(tmp[i], t);
      if (hit) { c[i] = tmp[i]; o[i] = OOB; }
      ok &= o[i] == OOB;
    }
    if (__all(ok)) break;
    if ((spins & 63u) == 0u && __builtin_amdgcn_s_memrealtime() > deadline) break;
    if (SLEEP) __builtin_amdgcn_s_sleep(SLEEP);
  }
  asm volatile("" ::: "memory");
}
__device__ __forceinline__ void lds_wait_ge(LAS char* L, int word, unsigned want, unsigned long long deadline) {
  volatile LAS unsigned* p = (volatile LAS unsigned*)(L + L_FLAG) + word;
  for (unsigned spins = 1; (unsigned)__builtin_amdgcn_readfirstlane(*p) < want; ++spins) { __builtin_amdgcn_s_sleep(1); if ((spins & 1023u) == 0u && __builtin_amdgcn_s_memrealtime() > deadline) break; }
  asm volatile("" ::: "memory");
}
__device__ __forceinline__ void lds_signal(LAS char* L, int word, int lane) {
  asm volatile("s_waitcnt lgkmcnt(0)" ::: "memory");
  if (lane == 0) (void)__hip_atomic_fetch_add((LAS unsigned*)(L + L_FLAG) + word, 1u, __ATOMIC_RELAXED, __HIP_MEMORY_SCOPE_WORKGROUP);
  asm volatile("" ::: "memory");
}
__device__ __forceinline__ unsigned lds_arrive(LAS char* L, int word, int lane) {
  asm volatile("s_waitcnt lgkmcnt(0)" ::: "memory");
  unsigned old = 0;
  if (lane == 0) old = __hip_atomic_fetch_add((LAS unsigned*)(L + L_FLAG) + word, 1u, __ATOMIC_RELAXED, __HIP_MEMORY_SCOPE_WORKGROUP);
  old = __builtin_amdgcn_readfirstlane(old);
  asm volatile("" ::: "memory");
  return old;
}
__device__ __forceinline__ unsigned pack2(unsigned lo, unsigned hi) { return (lo >> 16) | (hi & 0xffff0000u); }
template <int N> __device__ __forceinline__ unsigned shl_row(unsigned x) { return (unsigned)__builtin_amdgcn_mov_dpp((int)x, 0x100 + N, 0xf, 0xf, true); }
__device__ __forceinline__ void wg_barrier() {
  asm volatile("s_waitcnt lgkmcnt(0)" ::: "memory");
  __builtin_amdgcn_s_barrier();
  asm volatile("" ::: "memory");
}
__device__ __forceinline__ void dma16(const char* gsrc, unsigned lds_dst) {
  unsigned keep;
  asm volatile("s_mov_b32 %0, m0\n\ts_mov_b32 m0, %2\n\ts_nop 0\n\tglobal_load_lds_dwordx4 %1, off nt\n\ts_mov_b32 m0, %0"
               : "=&s"(keep) : "v"(gsrc), "s"(lds_dst) : "memory");
}
#define VM_DRAIN() asm volatile("s_waitcnt vmcnt(0)" ::: "memory")

#define STAMP(i) do { __builtin_amdgcn_sched_barrier(0); stampv[i] = __builtin_amdgcn_s_memrealtime(); __builtin_amdgcn_sched_barrier(0); } while (0)
#define STAMP_ARG , unsigned long long (&stampv)[24]
#define STAMP_PASS , stampv

template <int NL> __device__ __forceinline__ int piece_off(int wbase, int rot, int i) {
  int r = i + rot; r = r >= NL ? r - NL : r;
  return wbase + r * 4096;
}

__device__ __forceinline__ float wg_scale(LAS char* L, float m, float& M, unsigned long long deadline) {
  lds_wait_ge(L, FL_M, 8u, deadline);
  const f32x4 a = *(LAS f32x4*)(L + L_ML), c = *(LAS f32x4*)(L + L_ML + 16);
  M = fmaxf(fmaxf(fmaxf(a[0], a[1]), fmaxf(a[2], a[3])), fmaxf(fmaxf(c[0], c[1]), fmaxf(c[2], c[3])));
  return __builtin_amdgcn_exp2f((m - M) * L2E);
}

template <int NL, int TRIG>
__device__ __forceinline__ void attend(rsrc_t rk, rsrc_t rv, int voff, int wbase, int rot, v4u (&a)[WIN], v4u q, LAS char* L, int w, int lane,
                                       float& M, unsigned long long deadline STAMP_ARG) {
  float s[NL];
#pragma unroll
  for (int i = 0; i < NL; ++i) {
    const v4u kk = a[i % WIN];
    float d0 = dot2(kk.x, q.x, 0.f), d1 = dot2(kk.y, q.y, 0.f);
    d0 = dot2(kk.z, q.z, d0); d1 = dot2(kk.w, q.w, d1);
    s[i] = row16_sum(d0 + d1);
    asm volatile("" : "+v"(s[i]));
    const int n = i + WIN;
    if (n < NL) a[i % WIN] = bl128<AUX_NT>(rk, voff, piece_off<NL>(wbase, rot, n));
    else a[i % WIN] = bl128<AUX_NT>(rv, voff, piece_off<NL>(wbase, rot, n - NL));
    if (i == 2 * NL - WIN - 1 - TRIG) { if (w >= 4) lds_signal(L, FL_V, lane); }
    __builtin_amdgcn_sched_barrier(0);
  }
  STAMP(5);
  float m = -INFINITY;
#pragma unroll
  for (int i = 0; i < NL; i += 2) {
    const unsigned p1 = pkbf(s[i], s[i + 1]);
    const unsigned p2 = pkbf(blo(p1) * SCALE, bhi(p1) * SCALE);
    s[i] = blo(p2); s[i + 1] = bhi(p2);
    m = fmaxf(m, fmaxf(s[i], s[i + 1]));
  }
  m = x32_max(x16_max(m));
  if (lane == 0) *(LAS float*)(L + L_ML + w * 4) = m;
  lds_signal(L, FL_M, lane);
  const float mb = -m * L2E;
  float l = 0.f;
#pragma unroll
  for (int i = 0; i < NL; ++i) { s[i] = __builtin_amdgcn_exp2f(fmaf(s[i], L2E, mb)); l += s[i]; }
  l = x32_sum(x16_sum(l));
  float acc[8], e = 0.f;
#pragma unroll
  for (int k = 0; k < 8; ++k) acc[k] = 0.f;
  __builtin_amdgcn_sched_barrier(0);
#pragma unroll
  for (int i = 0; i < NL; ++i) {
    const v4u vv = a[(NL + i) % WIN];
    const float p = s[i];
    acc[0] = fmaf(p, blo(vv.x), acc[0]); acc[1] = fmaf(p, bhi(vv.x), acc[1]);
    acc[2] = fmaf(p, blo(vv.y), acc[2]); acc[3] = fmaf(p, bhi(vv.y), acc[3]);
    acc[4] = fmaf(p, blo(vv.z), acc[4]); acc[5] = fmaf(p, bhi(vv.z), acc[5]);
    acc[6] = fmaf(p, blo(vv.w), acc[6]); acc[7] = fmaf(p, bhi(vv.w), acc[7]);
    asm volatile("" : "+v"(acc[0]), "+v"(acc[1]), "+v"(acc[2]), "+v"(acc[3]), "+v"(acc[4]), "+v"(acc[5]), "+v"(acc[6]), "+v"(acc[7]));
    const int n = i + WIN;
    if (n < NL) a[(NL + i) % WIN] = bl128<AUX_NT>(rv, voff, piece_off<NL>(wbase, rot, n));
    if (NL + i == 2 * NL - WIN - 1 - TRIG) { if (w >= 4) lds_signal(L, FL_V, lane); }
    if (i == NL / 2) { if (w >= 4) e = wg_scale(L, m, M, deadline); }
    __builtin_amdgcn_sched_barrier(0);
  }
  STAMP(6);
  if (w < 4) e = wg_scale(L, m, M, deadline);
  float r4[4];
#pragma unroll
  for (int k = 0; k < 4; ++k) { auto t = __builtin_amdgcn_permlane32_swap(f2u(acc[k]), f2u(acc[k + 4]), false, false); r4[k] = u2f(t[0]) + u2f(t[1]); }
  auto t0 = __builtin_amdgcn_permlane16_swap(f2u(r4[0]), f2u(r4[1]), false, false);
  auto t1 = __builtin_amdgcn_permlane16_swap(f2u(r4[2]), f2u(r4[3]), false, false);
  const int d = 8 * (lane & 15) + ((lane >> 4) & 1) + 4 * (lane >> 5);
  *(LAS float*)(L + L_OW + w * 512 + d * 4) = e * (u2f(t0[0]) + u2f(t0[1]));
  *(LAS float*)(L + L_OW + w * 512 + d * 4 + 8) = e * (u2f(t1[0]) + u2f(t1[1]));
  if (lane == 0) *(LAS float*)(L + L_ML + 32 + w * 4) = e * l;
}

template <int s>
__device__ __forceinline__ void body(const Params& p, LAS char* L) {
  const unsigned Lb = (unsigned)(uintptr_t)L;
  const int tid = threadIdx.x, lane = tid & 63, w = __builtin_amdgcn_readfirstlane(tid >> 6);
  const int g = blockIdx.x, idx = g >> 1, xp = idx & 3, slot = idx >> 2;
  const int h = xp * 8 + (slot & 7), b = slot >> 3;
  const int lg = lane >> 4, li = lane & 15;
  unsigned long long stampv[24];
#pragma unroll
  for (int i = 0; i < 24; ++i) stampv[i] = 0;
  const unsigned long long cyc0 = __builtin_amdgcn_s_memtime();
  const unsigned long long t_start = __builtin_amdgcn_s_memrealtime();
  const unsigned long long deadline = t_start + 2000000ull;
  STAMP(0);
  if (tid < 16) ((LAS unsigned*)(L + L_FLAG))[tid] = 0u;
  wg_barrier();
  const rsrc_t rw = mkrs(p.ws, WS_END);
  const int jq = 2 * b + s;
  unsigned xr[5];
  if (w < 4) {
    const rsrc_t rx = mkrs(p.x + w * 1280, 1280);
#pragma unroll
    for (int i = 0; i < 5; ++i) xr[i] = __builtin_amdgcn_raw_buffer_load_b32(rx, lane * 4 + i * 256, 0, 0);
    __builtin_amdgcn_sched_barrier(0);
#pragma unroll
    for (int i = 0; i < 5; ++i) {
      const int n = w * 5 + i;
      dma16(p.wq + (size_t)(80 * jq + 4 * n + lg) * 8192 + h * 256 + li * 16, Lb + L_WQ + n * 1024);
    }
  }
  const unsigned ev = __builtin_amdgcn_raw_buffer_load_b32(rw, WS_E, 0, AUX_SC1);
  __builtin_amdgcn_sched_barrier(0);
  constexpr int nl = s ? NLO : NLE;
  const size_t kvbase = ((size_t)(b * NH + h) * SK + (SK - WK) + (s ? 32 * NLE : 0)) * 256;
  const rsrc_t rk = mkrs(p.k + kvbase, (unsigned)nl * 8192), rv = mkrs(p.v + kvbase, (unsigned)nl * 8192);
  const int voff = lane * 16, wbase = ((w >> 2) * 4 * nl + (w & 3)) * 1024, rot = idx & 15;
  v4u a[WIN];
#pragma unroll
  for (int i = 0; i < KA; ++i) { a[i] = bl128<AUX_NT>(rk, voff, piece_off<nl>(wbase, rot, i)); __builtin_amdgcn_sched_barrier(0); }
  STAMP(1);
  Tags tg;
  if (w < 4) {
    asm volatile("" : "+v"(xr[0]), "+v"(xr[4]));
    STAMP(12);
    float xv[10]; float sum = 0.f;
#pragma unroll
    for (int i = 0; i < 5; ++i) { xv[2 * i] = blo(xr[i]); xv[2 * i + 1] = bhi(xr[i]); sum += xv[2 * i] + xv[2 * i + 1]; }
    const float mean = wave_sum(sum) * (1.0f / D);
    float sq = 0.f;
#pragma unroll
    for (int i = 0; i < 10; ++i) { const float dd = xv[i] - mean; sq = fmaf(dd, dd, sq); }
    const float var = rbf(wave_sum(sq) * (1.0f / D)), meanb = rbf(mean);
    const float rden = __builtin_amdgcn_rcpf(rbf(__builtin_amdgcn_sqrtf(rbf(var + 1e-5f))));
#pragma unroll
    for (int i = 0; i < 5; ++i) {
      const int id = lane + 64 * i - 40 * jq;
      const unsigned d2 = pkbf(xv[2 * i] - meanb, xv[2 * i + 1] - meanb);
      const unsigned pk = pkbf(blo(d2) * rden, bhi(d2) * rden);
      if (id >= 0 && id < 40) *(LAS unsigned*)(L + L_XN + w * 160 + id * 4) = pk;
    }
  }
  STAMP(3);
  {
    const unsigned u = __builtin_amdgcn_readfirstlane(ev) + 1u;
    tg.f0 = u & 0xfffu; tg.f1 = (u >> 12) & 0xfffu; tg.f2 = (u >> 24) | 0xa00u; tg.f3 = (~u) & 0xfffu;
    asm volatile("" : "+s"(tg.f0), "+s"(tg.f1), "+s"(tg.f2), "+s"(tg.f3) :: "memory");
  }
  STAMP(13);
  if (w < 4) {
    lds_signal(L, FL_QA, lane);
    lds_wait_ge(L, FL_QA, 4u, deadline);
    STAMP(14);
#pragma unroll
    for (int tt = 0; tt < 2; ++tt) {
      const int t = 2 * w + tt;
      f32x4 c = {0.f, 0.f, 0.f, 0.f};
#pragma unroll
      for (int ks = 0; ks < 5; ++ks) {
        const s16x4 af = trd(L, L_WQ + (16 * ks + 4 * lg + (li >> 2)) * 256 + (16 * t + 4 * (li & 3)) * 2);
        const s16x4 bf = *(LAS s16x4*)(L + L_XN + (li & 3) * 160 + (16 * ks + 4 * lg) * 2);
        c = __builtin_amdgcn_mfma_f32_16x16x16bf16_1k(af, bf, c, 0, 0, 0);
      }
      publish(rw, li < 4 ? WS_QP + (((h * 4 + li) * 8 + jq) * 32 + 4 * t + lg) * 16 : OOB, enc4(c[0], c[1], c[2], c[3], tg));
    }
  }
  STAMP(2);
#pragma unroll
  for (int i = KA; i < KB; ++i) { a[i] = bl128<AUX_NT>(rk, voff, piece_off<nl>(wbase, rot, i)); __builtin_amdgcn_sched_barrier(0); }
  if (w == 0) {
    int off[4]; v4u c[4];
#pragma unroll
    for (int i = 0; i < 4; ++i) off[i] = WS_QP + ((h * 4 + b) * 256 + 64 * i + lane) * 16;
    if (FK_QDELAY) { while (__builtin_amdgcn_s_memrealtime() < t_start + FK_QDELAY) __builtin_amdgcn_s_sleep(2); }
    STAMP(15);
    gather<4>(rw, off, c, tg, deadline);
    STAMP(16);
    float q0 = (dec(c[0].x) + dec(c[1].x)) + (dec(c[2].x) + dec(c[3].x)), q1 = (dec(c[0].y) + dec(c[1].y)) + (dec(c[2].y) + dec(c[3].y));
    float q2 = (dec(c[0].z) + dec(c[1].z)) + (dec(c[2].z) + dec(c[3].z)), q3 = (dec(c[0].w) + dec(c[1].w)) + (dec(c[2].w) + dec(c[3].w));
    q0 = x32_sum(q0); q1 = x32_sum(q1); q2 = x32_sum(q2); q3 = x32_sum(q3);
    if (lane < 32) *(LAS v2u*)(L + L_QF + lane * 8) = v2u{pkbf(q0, q1), pkbf(q2, q3)};
    lds_signal(L, FL_Q, lane);
  }
#pragma unroll
  for (int i = KB; i < WIN; ++i) { a[i] = bl128<AUX_NT>(rk, voff, piece_off<nl>(wbase, rot, i)); __builtin_amdgcn_sched_barrier(0); }
  lds_wait_ge(L, FL_Q, 1u, deadline);
  const v4u q = *(LAS v4u*)(L + L_QF + li * 16);
  STAMP(4);
  float M;
  attend<nl, s ? FK_TRIG_O : FK_TRIG_E>(rk, rv, voff, wbase, rot, a, q, L, w, lane, M, deadline STAMP_PASS);
  if (lds_arrive(L, FL_PART, lane) == 7u) {
    LAS char* ow = L + L_OW + (lane >> 5) * 2048 + (lane & 31) * 16;
    const f32x4 oa = *(LAS f32x4*)(ow), ob = *(LAS f32x4*)(ow + 512), oc = *(LAS f32x4*)(ow + 1024), od = *(LAS f32x4*)(ow + 1536);
    const f32x4 la = *(LAS f32x4*)(L + L_ML + 32), lb = *(LAS f32x4*)(L + L_ML + 48);
    float o[4];
#pragma unroll
    for (int k = 0; k < 4; ++k) o[k] = x32_sum((oa[k] + ob[k]) + (oc[k] + od[k]));
    const float lsum = ((la[0] + la[1]) + (la[2] + la[3])) + ((lb[0] + lb[1]) + (lb[2] + lb[3]));
    const int base = WS_OP + (h * 8 + jq) * 640;
    publish(rw, lane < 32 ? base + lane * 16 : (lane == 32 ? base + 512 : OOB),
            lane < 32 ? enc4(o[0], o[1], o[2], o[3], tg) : enc4(M, lsum, 0.f, 0.f, tg));
    lds_signal(L, FL_PUB, lane);
  }
  STAMP(8);
  int role = 0, strip = 0;
  if constexpr (s) role = 1;
  else if (slot < 14) { const int mm = xp + 4 * (slot >> 1); if (mm < 27) { role = 3; strip = 2 * mm + (slot & 1); } }
  else if (slot < 26) { const int sp = slot - 14, n = xp + 4 * (sp >> 2); if (n < 10) { role = 4; strip = 4 * n + (sp & 3); } }
  if (w < 4 && role != 0) {
    lds_wait_ge(L, s ? FL_V : FL_PUB, s ? 4u : 1u, deadline);
    if constexpr (s) {
#pragma unroll
      for (int i = 0; i < 10; ++i) {
        const int n = w * 10 + i, id = n * 64 + lane, r = id / 20, c = id - r * 20;
        dma16(p.wo + (size_t)(128 * h + r) * 1280 + b * 320 + c * 16, Lb + L_IMG + n * 1024);
      }
    } else if (role == 3) {
#pragma unroll
      for (int i = 0; i < 20; ++i) {
        const int n = w * 20 + i, r = n * 8 + (lane >> 3), c = lane & 7;
        dma16(((c & 4) ? p.w3 : p.w1) + (size_t)r * 3456 + strip * 64 + (c & 3) * 16, Lb + L_IMG + n * 1024);
      }
    } else {
#pragma unroll
      for (int i = 0; i < 14; ++i) {
        const int n = w + 4 * i, r = n * 32 + (lane >> 1);
        if (n < 54) dma16(p.w2 + (size_t)r * 1280 + strip * 32 + (lane & 1) * 16, Lb + L_IMG + n * 1024);
      }
    }
    VM_DRAIN();
  }
  STAMP(7);
  const int gl = (li >> 3) * 128 + (((li >> 2) & 1) * 4 + lg) * 16;
  if constexpr (s) {
    v4u af[12];
    int nt = 0, t0 = 0, apo[3];
    if (w < 4) {
      lds_signal(L, FL_DMA, lane);
      lds_wait_ge(L, FL_DMA, 4u, deadline);
      nt = w < 2 ? 3 : 2; t0 = w < 2 ? 3 * w : 2 * w + 2;
#pragma unroll
      for (int tt = 0; tt < 3; ++tt) {
        const int t = (tt < nt) ? t0 + tt : t0;
#pragma unroll
        for (int ks = 0; ks < 4; ++ks) af[tt * 4 + ks] = trfrag(L, L_IMG + (32 * ks + 8 * lg + (li >> 2)) * 320 + (16 * t + 4 * (li & 3)) * 2, 4 * 320);
        const int lc = 16 * t + 4 * lg, u = 8 * b + lc / 20, cc = (lc % 20) >> 2;
        apo[tt] = (li < 4 && tt < nt) ? WS_AP + (((li * 32 + u) * 5 + cc) * 32 + h) * 16 : OOB;
      }
    } else {
      const int bb = w - 4, half = lane >> 5;
      int off[2]; v4u c[2];
      off[0] = WS_OP + (h * 8 + 2 * bb + half) * 640 + (lane & 31) * 16;
      off[1] = WS_OP + (h * 8 + 2 * bb + half) * 640 + 512;
      gather<2, FK_SL0>(rw, off, c, tg, deadline);
      const float ms = dec(c[1].x), ls = dec(c[1].y);
      const float Mx = x32_max(ms), e = __builtin_amdgcn_exp2f((ms - Mx) * L2E), wgt = e * __builtin_amdgcn_rcpf(x32_sum(e * ls));
      const float o0 = x32_sum(wgt * dec(c[0].x)), o1 = x32_sum(wgt * dec(c[0].y)), o2 = x32_sum(wgt * dec(c[0].z)), o3 = x32_sum(wgt * dec(c[0].w));
      if (lane < 32) *(LAS v2u*)(L + L_OB + bb * 256 + lane * 8) = v2u{pkbf(o0, o1), pkbf(o2, o3)};
    }
    STAMP(9);
    wg_barrier();
    if (w < 4) {
      STAMP(17);
      v4u bfr[4];
#pragma unroll
      for (int ks = 0; ks < 4; ++ks) bfr[ks] = *(LAS v4u*)(L + L_OB + (li & 3) * 256 + ks * 64 + lg * 16);
      f32x4 c[3];
#pragma unroll
      for (int tt = 0; tt < 3; ++tt) c[tt] = f32x4{0.f, 0.f, 0.f, 0.f};
#pragma unroll
      for (int ks = 0; ks < 4; ++ks)
#pragma unroll
        for (int tt = 0; tt < 3; ++tt) c[tt] = mfma32(af[tt * 4 + ks], bfr[ks], c[tt]);
#pragma unroll
      for (int tt = 0; tt < 3; ++tt) publish(rw, apo[tt], enc4(c[tt][0], c[tt][1], c[tt][2], c[tt][3], tg));
    } else if (w < 7) {
      const int cc = 2 * (w - 4) + (lane >> 5);
      const bool on = cc < 5, mine = on && (lane & 31) == 0;
      const rsrc_t rx = mkrs(p.x, 4 * 1280);
      const v2u xx = __builtin_amdgcn_raw_buffer_load_b64(rx, mine ? b * 1280 + (20 * h + 4 * cc) * 2 : OOB, 0, 0);
      int off[1]; v4u c[1];
      off[0] = on ? WS_AP + (b * 32 + h) * 2560 + cc * 512 + (lane & 31) * 16 : OOB;
      gather<1, FK_SL1>(rw, off, c, tg, deadline);
      STAMP(10);
      const float r0 = rbf(x16_sum(row16_sum(dec(c[0].x)))), r1 = rbf(x16_sum(row16_sum(dec(c[0].y))));
      const float r2 = rbf(x16_sum(row16_sum(dec(c[0].z)))), r3 = rbf(x16_sum(row16_sum(dec(c[0].w))));
      const unsigned h01 = pkbf(blo(xx.x) + r0, bhi(xx.x) + r1), h23 = pkbf(blo(xx.y) + r2, bhi(xx.y) + r3);
      publish(rw, mine ? WS_H + (b * 160 + 5 * h + cc) * 16 : OOB, enc4(blo(h01), bhi(h01), blo(h23), bhi(h23), tg));
    }
    STAMP(11);
  } else if (role == 3) {
    wg_barrier();
    const int pt = w & 1, kq = w >> 1, np = kq < 2 ? 3 : 2;
    v4u ag[6], au[6];
#pragma unroll
    for (int i = 0; i < 6; ++i) {
      const int ks = 2 * ((i >> 1) < np ? kq + 4 * (i >> 1) : kq) + (i & 1);
      const int ro = L_IMG + (32 * ks + 4 * lg + (li >> 2)) * 128 + pt * 32 + (li & 3) * 8;
      ag[i] = trfrag(L, ro, 16 * 128); au[i] = trfrag(L, ro + 64, 16 * 128);
    }
    int off[3]; v4u c[3];
#pragma unroll
    for (int i = 0; i < 3; ++i) off[i] = i < np ? WS_H + (li & 3) * 2560 + (kq + 4 * i) * 256 + gl : OOB;
    STAMP(9);
    gather<3, FK_SL2>(rw, off, c, tg, deadline);
    STAMP(10);
    f32x4 cg = {0.f, 0.f, 0.f, 0.f}, cu = {0.f, 0.f, 0.f, 0.f};
    float s1 = 0.f, s2 = 0.f;
#pragma unroll
    for (int i = 0; i < 3; ++i) {
      if (i < np) {
        const unsigned p0 = pack2(c[i].x, c[i].y), p1 = pack2(c[i].z, c[i].w);
        unsigned p2 = shl_row<4>(p0), p3 = shl_row<4>(p1), p4 = shl_row<8>(p0), p5 = shl_row<8>(p1), p6 = shl_row<12>(p0), p7 = shl_row<12>(p1);
        asm volatile("" : "+v"(p2), "+v"(p3), "+v"(p4), "+v"(p5), "+v"(p6), "+v"(p7));
        const bool one = li >= 4;
        constexpr unsigned ONE2 = 0x3f803f80u;
        const v4u b0 = {one ? ONE2 : p0, one ? ONE2 : p1, one ? ONE2 : p2, one ? ONE2 : p3};
        const v4u b1 = {one ? ONE2 : p4, one ? ONE2 : p5, one ? ONE2 : p6, one ? ONE2 : p7};
        cg = mfma32(ag[2 * i], b0, cg); cu = mfma32(au[2 * i], b0, cu);
        cg = mfma32(ag[2 * i + 1], b1, cg); cu = mfma32(au[2 * i + 1], b1, cu);
        const float v0 = dec(c[i].x), v1 = dec(c[i].y), v2 = dec(c[i].z), v3 = dec(c[i].w);
        s1 += (v0 + v1) + (v2 + v3);
        s2 = fmaf(v0, v0, fmaf(v1, v1, fmaf(v2, v2, fmaf(v3, v3, s2))));
      }
    }
    s1 += dpp<0x124>(s1); s2 += dpp<0x124>(s2); s1 += dpp<0x128>(s1); s2 += dpp<0x128>(s2);
    s1 = x32_sum(x16_sum(s1)); s2 = x32_sum(x16_sum(s2));
    if (li < 5) {
      *(LAS f32x4*)(L + L_RED + (w * 2 + 0) * 320 + (lg * 5 + li) * 16) = cg;
      *(LAS f32x4*)(L + L_RED + (w * 2 + 1) * 320 + (lg * 5 + li) * 16) = cu;
    }
    if (lane < 4 && pt == 0) *(LAS v2u*)(L + L_ST + (kq * 4 + lane) * 8) = v2u{f2u(s1), f2u(s2)};
    STAMP(18);
    wg_barrier();
    STAMP(19);
    if (w == 0) {
      const int p2 = (lane >> 4) & 1, e = lane & 15, eg = e >> 2, eb = e & 3;
      f32x4 gs = {0.f, 0.f, 0.f, 0.f}, us = gs, gc = gs, uc = gs;
      float t1 = 0.f, t2 = 0.f;
#pragma unroll
      for (int k = 0; k < 4; ++k) {
        LAS char* rb_ = L + L_RED + ((k * 2 + p2) * 2) * 320 + eg * 80;
        gs += *(LAS f32x4*)(rb_ + eb * 16); gc += *(LAS f32x4*)(rb_ + 64);
        us += *(LAS f32x4*)(rb_ + 320 + eb * 16); uc += *(LAS f32x4*)(rb_ + 320 + 64);
        const v2u st = *(LAS v2u*)(L + L_ST + (k * 4 + eb) * 8); t1 += u2f(st.x); t2 += u2f(st.y);
      }
      const float mean = t1 * (1.0f / D), var = rbf(fmaxf(fmaf(-mean, mean, t2 * (1.0f / D)), 0.f)), meanb = rbf(mean);
      const float rden = __builtin_amdgcn_rcpf(rbf(__builtin_amdgcn_sqrtf(rbf(var + 1e-5f))));
      float av[4];
#pragma unroll
      for (int k = 0; k < 4; ++k) {
        const float gg = rbf(rden * fmaf(-meanb, gc[k], gs[k])), uu = rbf(rden * fmaf(-meanb, uc[k], us[k]));
        const float si = rbf(gg * __builtin_amdgcn_rcpf(1.0f + __builtin_amdgcn_exp2f(-gg * L2E)));
        av[k] = rbf(si * uu);
      }
      publish(rw, lane < 32 ? WS_ACT + (eb * 432 + 8 * strip + 4 * p2 + eg) * 16 : OOB, enc4(av[0], av[1], av[2], av[3], tg));
    }
    STAMP(11);
  } else if (role == 4) {
    wg_barrier();
    const int np = w < 3 ? 4 : 3;
    v4u af[8];
#pragma unroll
    for (int i = 0; i < 8; ++i) {
      const int ks = 2 * ((i >> 1) < np ? w + 8 * (i >> 1) : w) + (i & 1);
      af[i] = trfrag(L, L_IMG + (32 * ks + 4 * lg + (li >> 2)) * 32 + (li & 3) * 8, 16 * 32);
    }
    v4u hc[1];
    if (w == 0) {
      int off[1]; off[0] = lane < 16 ? WS_H + ((lane & 3) * 160 + 4 * strip + (lane >> 2)) * 16 : OOB;
      gather<1, FK_SL2>(rw, off, hc, tg, deadline);
    }
    int off[4]; v4u c[4];
#pragma unroll
    for (int i = 0; i < 4; ++i) off[i] = i < np ? WS_ACT + (li & 3) * 6912 + (w + 8 * i) * 256 + gl : OOB;
    STAMP(9);
    gather<4, FK_SL3>(rw, off, c, tg, deadline);
    STAMP(10);
    f32x4 cc = {0.f, 0.f, 0.f, 0.f};
#pragma unroll
    for (int i = 0; i < 4; ++i) {
      if (i < np) {
        const unsigned p0 = pack2(c[i].x, c[i].y), p1 = pack2(c[i].z, c[i].w);
        cc = mfma32(af[2 * i], v4u{p0, p1, shl_row<4>(p0), shl_row<4>(p1)}, cc);
        cc = mfma32(af[2 * i + 1], v4u{shl_row<8>(p0), shl_row<8>(p1), shl_row<12>(p0), shl_row<12>(p1)}, cc);
      }
    }
    if (li < 4) *(LAS f32x4*)(L + L_RED + w * 256 + (lg * 4 + li) * 16) = cc;
    STAMP(18);
    wg_barrier();
    STAMP(19);
    if (w == 0) {
      f32x4 sm = {0.f, 0.f, 0.f, 0.f};
#pragma unroll
      for (int k = 0; k < 8; ++k) sm += *(LAS f32x4*)(L + L_RED + k * 256 + (lane & 15) * 16);
      const v2u o = {pkbf(dec(hc[0].x) + rbf(sm[0]), dec(hc[0].y) + rbf(sm[1])), pkbf(dec(hc[0].z) + rbf(sm[2]), dec(hc[0].w) + rbf(sm[3]))};
      const rsrc_t ro = mkrs(p.out, 4 * 1280);
      __builtin_amdgcn_raw_buffer_store_b64(o, ro, lane < 16 ? (lane & 3) * 1280 + (16 * strip + 4 * (lane >> 2)) * 2 : OOB, 0, AUX_SC1);
      if (strip == 0) __builtin_amdgcn_raw_buffer_store_b32(__builtin_amdgcn_readfirstlane(ev) + 1u, rw, lane == 0 ? WS_E : OOB, 0, AUX_SC1);
    }
    STAMP(11);
  }
  STAMP(23);
  if (lane == 0) {
    unsigned long long* st = (unsigned long long*)(p.ws + WS_DIAG) + (size_t)(g * 8 + w) * 32;
#pragma unroll
    for (int i = 0; i < 24; ++i) st[i] = stampv[i];
    st[31] = __builtin_amdgcn_s_memtime() - cyc0;
    st[29] = role; st[30] = __builtin_amdgcn_s_getreg((20 << 0) | (0 << 6) | (3 << 11));
  }
}

__global__ void __launch_bounds__(512, 2) fused_block(Params p) {
  extern __shared__ __attribute__((aligned(16))) char smem[];
  if (blockIdx.x & 1) body<1>(p, (LAS char*)smem);
  else body<0>(p, (LAS char*)smem);
}
}

extern "C" void kernel_launch(void* const* d_in, const int* in_sizes, int n_in, void* d_out, int out_size, void* d_ws, size_t ws_size,
                              hipStream_t stream) {
  (void)in_sizes; (void)n_in; (void)out_size; (void)ws_size;
  fk::Params p;
  p.x = (const char*)d_in[0]; p.k = (const char*)d_in[1]; p.v = (const char*)d_in[2]; p.wq = (const char*)d_in[3]; p.wo = (const char*)d_in[4];
  p.w1 = (const char*)d_in[6]; p.w3 = (const char*)d_in[7]; p.w2 = (const char*)d_in[8];
  p.out = (char*)d_out; p.ws = (char*)d_ws;
  (void)hipFuncSetAttribute((const void*)fk::fused_block, hipFuncAttributeMaxDynamicSharedMemorySize, fk::L_END);
  fk::fused_block<<<256, 512, fk::L_END, stream>>>(p);
}
```

```cpp
#include <hip/hip_runtime.h>
#include <hip/hip_bf16.h>
#include <stdint.h>

namespace fk {
#define GAS __attribute__((address_space(1)))
#define LAS __attribute__((address_space(3)))
typedef unsigned v4u __attribute__((ext_vector_type(4)));
typedef unsigned v2u __attribute__((ext_vector_type(2)));
typedef float f32x4 __attribute__((ext_vector_type(4)));
typedef short s16x4 __attribute__((ext_vector_type(4)));
typedef __bf16 bf16x8 __attribute__((ext_vector_type(8)));
typedef __bf16 bf16x2 __attribute__((ext_vector_type(2)));
typedef __amdgpu_buffer_rsrc_t rsrc_t;

constexpr int DELTA = 1;
constexpr int WIN = 12;
constexpr int KA = 6, KB = 8;
constexpr int QDELAY = 450;
constexpr int E3 = 3, E4 = 0;
constexpr int D1 = 80;
constexpr int D2 = 325;
constexpr int D3 = 495;

constexpr int D = 640, NH = 32, SK = 8192, WK = 2048;
constexpr int NLE = 32 + DELTA, NLO = 32 - DELTA;
constexpr int AUX_NT = 2, AUX_SC1 = 16;
constexpr int OOB = 0x7ffffff0;
constexpr float SCALE = 0.08838834764831845f, L2E = 1.4426950408889634f;

constexpr int WS_E = 0;
constexpr int WS_QP = 4096;
constexpr int WS_OP = WS_QP + 32 * 4 * 8 * 512;
constexpr int WS_AP = WS_OP + 32 * 8 * 640;
constexpr int WS_H = WS_AP + 4 * 32 * 160 * 16;
constexpr int WS_ACT = WS_H + 4 * 160 * 16;
constexpr int WS_B = WS_ACT + 4 * 432 * 16;
constexpr int WS_END = WS_B + 256 * 16;

constexpr int L_FLAG = 0;
constexpr int L_XN = 64;
constexpr int L_QF = L_XN + 640;
constexpr int L_ML = L_QF + 256;
constexpr int L_OW = L_ML + 64;
constexpr int L_OB = L_OW + 4096;
constexpr int L_RED = L_OB + 1024;
constexpr int L_ST = L_RED + 5120;
constexpr int L_WQ = L_ST + 256;
constexpr int L_IMG = L_WQ + 20480;
constexpr int L_END = L_IMG + 81920;
static_assert(L_XN % 16 == 0 && L_QF % 16 == 0 && L_ML % 16 == 0 && L_OW % 16 == 0 && L_OB % 16 == 0 && L_RED % 16 == 0 && L_ST % 16 == 0 &&
              L_WQ % 16 == 0 && L_IMG % 16 == 0, "LDS alignment");
static_assert(L_END <= 160 * 1024 && L_END > 80 * 1024, "one workgroup per CU");
enum { FL_QA = 0, FL_Q = 1, FL_V = 2, FL_DMA = 3, FL_PART = 4, FL_PUB = 5, FL_M = 6, FL_T = 7, FL_TL = 8, FL_DI = 9 };

struct Params {
  const char* x; const char* k; const char* v; const char* wq; const char* wo; const char* w1; const char* w3; const char* w2;
  char* out; char* ws;
};

struct Tags { unsigned f0, f1, f2, f3, mask, low; };

__device__ __forceinline__ rsrc_t mkrs(const void* p, unsigned bytes) { return __builtin_amdgcn_make_buffer_rsrc((void*)p, 0, bytes, 0x00020000); }
template <int AUX> __device__ __forceinline__ v4u bl128(rsrc_t r, int voff, int soff) { return __builtin_amdgcn_raw_buffer_load_b128(r, voff, soff, AUX); }
__device__ __forceinline__ float u2f(unsigned u) { return __builtin_bit_cast(float, u); }
__device__ __forceinline__ unsigned f2u(float f) { return __builtin_bit_cast(unsigned, f); }
__device__ __forceinline__ unsigned pkbf(float lo, float hi) { bf16x2 p = {(__bf16)lo, (__bf16)hi}; return __builtin_bit_cast(unsigned, p); }
__device__ __forceinline__ float blo(unsigned p) { return u2f(p << 16); }
__device__ __forceinline__ float bhi(unsigned p) { return u2f(p & 0xffff0000u); }
__device__ __forceinline__ float rbf(float x) { return blo(pkbf(x, 0.f) & 0xffffu); }
__device__ __forceinline__ float dot2(unsigned a, unsigned b, float c) {
  return __builtin_amdgcn_fdot2_f32_bf16(__builtin_bit_cast(bf16x2, a), __builtin_bit_cast(bf16x2, b), c, false);
}
template <int CTRL> __device__ __forceinline__ float dpp(float x) {
  return u2f((unsigned)__builtin_amdgcn_mov_dpp((int)f2u(x), CTRL, 0xf, 0xf, true));
}
__device__ __forceinline__ float row16_sum(float x) {
  x += dpp<0x128>(x); x += dpp<0x124>(x); x += dpp<0x122>(x); x += dpp<0x121>(x); return x;
}
__device__ __forceinline__ float x16_sum(float x) {
  auto s = __builtin_amdgcn_permlane16_swap(f2u(x), f2u(x), false, false); return u2f(s[0]) + u2f(s[1]);
}
__device__ __forceinline__ float x32_sum(float x) {
  auto s = __builtin_amdgcn_permlane32_swap(f2u(x), f2u(x), false, false); return u2f(s[0]) + u2f(s[1]);
}
__device__ __forceinline__ float x16_max(float x) { auto s = __builtin_amdgcn_permlane16_swap(f2u(x), f2u(x), false, false); return fmaxf(u2f(s[0]), u2f(s[1])); }
__device__ __forceinline__ float x32_max(float x) { auto s = __builtin_amdgcn_permlane32_swap(f2u(x), f2u(x), false, false); return fmaxf(u2f(s[0]), u2f(s[1])); }
__device__ __forceinline__ float wave_sum(float x) { return x32_sum(x16_sum(row16_sum(x))); }
__device__ __forceinline__ f32x4 mfma32(v4u a, v4u b, f32x4 c) {
  return __builtin_amdgcn_mfma_f32_16x16x32_bf16(__builtin_bit_cast(bf16x8, a), __builtin_bit_cast(bf16x8, b), c, 0, 0, 0);
}
__device__ __forceinline__ s16x4 trd(LAS char* L, int off) { return __builtin_amdgcn_ds_read_tr16_b64_v4i16((LAS s16x4*)(L + off)); }
__device__ __forceinline__ v4u trfrag(LAS char* L, int off, int step) {
  const v2u a = __builtin_bit_cast(v2u, trd(L, off)), b = __builtin_bit_cast(v2u, trd(L, off + step));
  return v4u{a.x, a.y, b.x, b.y};
}
__device__ __forceinline__ unsigned enc1(unsigned v, unsigned f, unsigned m) { return (v & m) | (f & ~m); }
__device__ __forceinline__ v4u enc4(float a, float b, float c, float d, const Tags& t) {
  return v4u{enc1(f2u(a), t.f0, t.mask), enc1(f2u(b), t.f1, t.mask), enc1(f2u(c), t.f2, t.mask), enc1(f2u(d), t.f3, t.mask)};
}
__device__ __forceinline__ v4u encp(unsigned p01, unsigned p23, const Tags& t) {
  return v4u{(p01 << 16) | t.f0, enc1(p01, t.f1, 0xffff0000u), (p23 << 16) | t.f2, enc1(p23, t.f3, 0xffff0000u)};
}
__device__ __forceinline__ bool tagok(v4u c, const Tags& t) { return (((c.x ^ t.f0) | (c.y ^ t.f1) | (c.z ^ t.f2) | (c.w ^ t.f3)) & t.low) == 0u; }
__device__ __forceinline__ float dec(unsigned w) { return u2f(w & 0xfffff000u); }
__device__ __forceinline__ unsigned pack2(unsigned lo, unsigned hi) { return __builtin_amdgcn_perm(hi, lo, 0x07060302u); }
__device__ __forceinline__ void publish(rsrc_t rw, int off, v4u c) { __builtin_amdgcn_raw_buffer_store_b128(c, rw, off, 0, AUX_SC1); }
template <int N, int SLEEP = 0>
__device__ __forceinline__ void gather(rsrc_t rw, const int (&off)[N], v4u (&c)[N], const Tags& tg, unsigned long long deadline) {
  int o[N];
#pragma unroll
  for (int i = 0; i < N; ++i) { o[i] = off[i]; c[i] = v4u{0u, 0u, 0u, 0u}; }
  for (unsigned spins = 1;; ++spins) {
    asm volatile("" ::: "memory");
    v4u t[N];
#pragma unroll
    for (int i = 0; i < N; ++i) t[i] = bl128<AUX_SC1>(rw, o[i], 0);
    bool ok = true;
#pragma unroll
    for (int i = 0; i < N; ++i) {
      const bool hit = tagok(t[i], tg);
      c[i].x = hit ? t[i].x : c[i].x; c[i].y = hit ? t[i].y : c[i].y; c[i].z = hit ? t[i].z : c[i].z; c[i].w = hit ? t[i].w : c[i].w;
      o[i] = hit ? OOB : o[i];
      ok &= o[i] == OOB;
    }
    if (__all(ok)) break;
    if ((spins & 63u) == 0u && __builtin_amdgcn_s_memrealtime() > deadline) break;
    if (SLEEP) __builtin_amdgcn_s_sleep(SLEEP);
  }
  asm volatile("" ::: "memory");
}
__device__ __forceinline__ void lds_wait_ge(LAS char* L, int word, unsigned want, unsigned long long deadline) {
  volatile LAS unsigned* p = (volatile LAS unsigned*)(L + L_FLAG) + word;
  for (unsigned spins = 1; (unsigned)__builtin_amdgcn_readfirstlane(*p) < want; ++spins) {
    __builtin_amdgcn_s_sleep(1);
    if ((spins & 1023u) == 0u && __builtin_amdgcn_s_memrealtime() > deadline) break;
  }
  asm volatile("" ::: "memory");
}
__device__ __forceinline__ void wait_until(unsigned long long t) { while (__builtin_amdgcn_s_memrealtime() < t) __builtin_amdgcn_s_sleep(2); }
__device__ __forceinline__ void lds_signal(LAS char* L, int word, int lane) {
  (void)lane;
  unsigned long long keep_exec;
  asm volatile("s_waitcnt lgkmcnt(0)\n\ts_mov_b64 %0, exec\n\ts_mov_b64 exec, 1\n\tds_add_u32 %1, %2\n\ts_mov_b64 exec, %0"
               : "=&s"(keep_exec) : "v"((unsigned)(uintptr_t)(L + L_FLAG + 4 * word)), "v"(1u) : "memory");
}
__device__ __forceinline__ unsigned lds_arrive(LAS char* L, int word, int lane) {
  (void)lane;
  unsigned long long keep_exec; unsigned old;
  asm volatile("s_waitcnt lgkmcnt(0)\n\ts_mov_b64 %0, exec\n\ts_mov_b64 exec, 1\n\tds_add_rtn_u32 %1, %2, %3\n\ts_waitcnt lgkmcnt(0)\n\ts_mov_b64 exec, %0"
               : "=&s"(keep_exec), "=&v"(old) : "v"((unsigned)(uintptr_t)(L + L_FLAG + 4 * word)), "v"(1u) : "memory");
  return __builtin_amdgcn_readfirstlane(old);
}
template <int J> __device__ __forceinline__ unsigned qbc(unsigned x) { return (unsigned)__builtin_amdgcn_mov_dpp((int)x, J * 0x55, 0xf, 0xf, true); }
__device__ __forceinline__ void wg_barrier() {
  asm volatile("s_waitcnt lgkmcnt(0)" ::: "memory");
  __builtin_amdgcn_s_barrier();
  asm volatile("" ::: "memory");
}
template <int SLEEP>
__device__ __forceinline__ void post_tlast(LAS char* L, rsrc_t rw, int lane, const Tags& tg, unsigned long long t_start, unsigned long long deadline) {
  int off[4]; v4u c[4];
#pragma unroll
  for (int i = 0; i < 4; ++i) off[i] = WS_B + (64 * i + lane) * 16;
  gather<4, SLEEP>(rw, off, c, tg, deadline);
  const unsigned t0 = (unsigned)t_start - 4096u;
  unsigned d = 0;
#pragma unroll
  for (int i = 0; i < 4; ++i) d = max(d, ((c[i].x >> 12) - t0) & 0xfffffu);
  float f = (float)d;
  f = fmaxf(f, dpp<0x128>(f)); f = fmaxf(f, dpp<0x124>(f)); f = fmaxf(f, dpp<0x122>(f)); f = fmaxf(f, dpp<0x121>(f));
  f = x32_max(x16_max(f));
  unsigned tl = t0 + (unsigned)f;
  const unsigned now = (unsigned)__builtin_amdgcn_s_memrealtime();
  tl = (int)(tl - now) > 0 ? now : tl;
  if (lane == 0) *(LAS unsigned*)(L + L_FLAG + 4 * FL_TL) = tl;
  lds_signal(L, FL_T, lane);
}
__device__ __forceinline__ void wait_due(LAS char* L, unsigned ticks, unsigned long long deadline) {
  lds_wait_ge(L, FL_T, 1u, deadline);
  const unsigned due = __builtin_amdgcn_readfirstlane(*(volatile LAS unsigned*)(L + L_FLAG + 4 * FL_TL)) + ticks;
  while ((int)((unsigned)__builtin_amdgcn_s_memrealtime() - due) < 0) __builtin_amdgcn_s_sleep(1);
}
__device__ __forceinline__ void dma16(const char* gsrc, unsigned lds_dst) {
  unsigned keep_m0;
  asm volatile("s_mov_b32 %0, m0\n\ts_mov_b32 m0, %2\n\ts_nop 0\n\tglobal_load_lds_dwordx4 %1, off nt\n\ts_mov_b32 m0, %0"
               : "=&s"(keep_m0) : "v"(gsrc), "s"(lds_dst) : "memory");
}
#define VM_DRAIN() asm volatile("s_waitcnt vmcnt(0)" ::: "memory")

template <int NL> __device__ __forceinline__ int piece_off(int wbase, int rot, int i) {
  int r = i + rot; r = r >= NL ? r - NL : r;
  return wbase + r * 4096;
}

__device__ __forceinline__ float wg_scale(LAS char* L, float m, float& M, unsigned long long deadline) {
  lds_wait_ge(L, FL_M, 8u, deadline);
  const f32x4 a = *(LAS f32x4*)(L + L_ML), c = *(LAS f32x4*)(L + L_ML + 16);
  M = fmaxf(fmaxf(fmaxf(a[0], a[1]), fmaxf(a[2], a[3])), fmaxf(fmaxf(c[0], c[1]), fmaxf(c[2], c[3])));
  return __builtin_amdgcn_exp2f((m - M) * L2E);
}

template <int NL>
__device__ __forceinline__ void attend(rsrc_t rk, rsrc_t rv, int voff, int wbase, int rot, v4u (&a)[WIN], v4u q, LAS char* L, int w, int lane,
                                       float& M, unsigned long long deadline) {
  constexpr int LASTV = 2 * NL - WIN - 1;
  float s[NL];
#pragma unroll
  for (int i = 0; i < NL; ++i) {
    const v4u kk = a[i % WIN];
    float d0 = dot2(kk.x, q.x, 0.f), d1 = dot2(kk.y, q.y, 0.f);
    d0 = dot2(kk.z, q.z, d0); d1 = dot2(kk.w, q.w, d1);
    s[i] = row16_sum(d0 + d1);
    asm volatile("" : "+v"(s[i]));
    const int n = i + WIN;
    if (n < NL) a[i % WIN] = bl128<AUX_NT>(rk, voff, piece_off<NL>(wbase, rot, n));
    else a[i % WIN] = bl128<AUX_NT>(rv, voff, piece_off<NL>(wbase, rot, n - NL));
    if (i == LASTV) { if (w >= 4) lds_signal(L, FL_V, lane); }
    __builtin_amdgcn_sched_barrier(0);
  }
  float m = -INFINITY;
#pragma unroll
  for (int i = 0; i < NL; i += 2) {
    const int i1 = i + 1 < NL ? i + 1 : i;
    const unsigned p1 = pkbf(s[i], s[i1]);
    const unsigned p2 = pkbf(blo(p1) * SCALE, bhi(p1) * SCALE);
    s[i] = blo(p2); s[i1] = bhi(p2);
    m = fmaxf(m, fmaxf(s[i], s[i1]));
  }
  m = x32_max(x16_max(m));
  if (lane == 0) *(LAS float*)(L + L_ML + w * 4) = m;
  lds_signal(L, FL_M, lane);
  const float mb = -m * L2E;
  float l = 0.f;
#pragma unroll
  for (int i = 0; i < NL; ++i) { s[i] = __builtin_amdgcn_exp2f(fmaf(s[i], L2E, mb)); l += s[i]; }
  l = x32_sum(x16_sum(l));
  float acc[8], e = 0.f;
#pragma unroll
  for (int k = 0; k < 8; ++k) acc[k] = 0.f;
  __builtin_amdgcn_sched_barrier(0);
#pragma unroll
  for (int i = 0; i < NL; ++i) {
    const v4u vv = a[(NL + i) % WIN];
    const float p = s[i];
    acc[0] = fmaf(p, blo(vv.x), acc[0]); acc[1] = fmaf(p, bhi(vv.x), acc[1]);
    acc[2] = fmaf(p, blo(vv.y), acc[2]); acc[3] = fmaf(p, bhi(vv.y), acc[3]);
    acc[4] = fmaf(p, blo(vv.z), acc[4]); acc[5] = fmaf(p, bhi(vv.z), acc[5]);
    acc[6] = fmaf(p, blo(vv.w), acc[6]); acc[7] = fmaf(p, bhi(vv.w), acc[7]);
    asm volatile("" : "+v"(acc[0]), "+v"(acc[1]), "+v"(acc[2]), "+v"(acc[3]), "+v"(acc[4]), "+v"(acc[5]), "+v"(acc[6]), "+v"(acc[7]));
    const int n = i + WIN;
    if (n < NL) a[(NL + i) % WIN] = bl128<AUX_NT>(rv, voff, piece_off<NL>(wbase, rot, n));
    if (NL + i == LASTV) { if (w >= 4) lds_signal(L, FL_V, lane); }
    if (i == 2) { if (w >= 4) e = wg_scale(L, m, M, deadline); }
    __builtin_amdgcn_sched_barrier(0);
  }
  if (w < 4) e = wg_scale(L, m, M, deadline);
  float r4[4];
#pragma unroll
  for (int k = 0; k < 4; ++k) { auto t = __builtin_amdgcn_permlane32_swap(f2u(acc[k]), f2u(acc[k + 4]), false, false); r4[k] = u2f(t[0]) + u2f(t[1]); }
  auto t0 = __builtin_amdgcn_permlane16_swap(f2u(r4[0]), f2u(r4[1]), false, false);
  auto t1 = __builtin_amdgcn_permlane16_swap(f2u(r4[2]), f2u(r4[3]), false, false);
  const int d = 8 * (lane & 15) + ((lane >> 4) & 1) + 4 * (lane >> 5);
  *(LAS float*)(L + L_OW + w * 512 + d * 4) = e * (u2f(t0[0]) + u2f(t0[1]));
  *(LAS float*)(L + L_OW + w * 512 + d * 4 + 8) = e * (u2f(t1[0]) + u2f(t1[1]));
  if (lane == 0) *(LAS float*)(L + L_ML + 32 + w * 4) = e * l;
}

__device__ __forceinline__ void ln_consts(float s1, float s2, float& meanb, float& rden) {
  const float mean = s1 * (1.0f / D), var = rbf(fmaxf(fmaf(-mean, mean, s2 * (1.0f / D)), 0.f));
  meanb = rbf(mean);
  rden = __builtin_amdgcn_rcpf(rbf(__builtin_amdgcn_sqrtf(rbf(var + 1e-5f))));
}
constexpr unsigned ONE2 = 0x3f803f80u;

template <int s>
__device__ __forceinline__ void body(const Params& p, LAS char* L) {
  const unsigned Lb = (unsigned)(uintptr_t)L;
  const int tid = threadIdx.x, lane = tid & 63, w = __builtin_amdgcn_readfirstlane(tid >> 6);
  const int g = blockIdx.x, idx = g >> 1, xp = idx & 3, slot = idx >> 2;
  const int h = xp * 8 + (slot & 7), b = slot >> 3;
  const int lg = lane >> 4, li = lane & 15;
  const unsigned long long t_start = __builtin_amdgcn_s_memrealtime();
  const unsigned long long deadline = t_start + 2000000ull;
  if (tid < 16) ((LAS unsigned*)(L + L_FLAG))[tid] = 0u;
  wg_barrier();
  const rsrc_t rw = mkrs(p.ws, WS_END);
  const int jq = 2 * b + s;
  unsigned xr[5];
  if (w < 4) {
    const rsrc_t rx = mkrs(p.x + w * 1280, 1280);
#pragma unroll
    for (int i = 0; i < 5; ++i) xr[i] = __builtin_amdgcn_raw_buffer_load_b32(rx, lane * 4 + i * 256, 0, 0);
    __builtin_amdgcn_sched_barrier(0);
#pragma unroll
    for (int i = 0; i < 5; ++i) {
      const int n = w * 5 + i, r = 4 * n + lg;
      dma16(p.wq + (size_t)(80 * jq + r) * 8192 + h * 256 + (((li >> 1) ^ (r & 7)) * 2 + (li & 1)) * 16, Lb + L_WQ + n * 1024);
    }
  }
  const unsigned ev = __builtin_amdgcn_raw_buffer_load_b32(rw, WS_E, 0, AUX_SC1);
  __builtin_amdgcn_sched_barrier(0);
  constexpr int nl = s ? NLO : NLE;
  const size_t kvbase = ((size_t)(b * NH + h) * SK + (SK - WK) + (s ? 32 * NLE : 0)) * 256;
  const rsrc_t rk = mkrs(p.k + kvbase, (unsigned)nl * 8192), rv = mkrs(p.v + kvbase, (unsigned)nl * 8192);
  const int voff = lane * 16, wbase = ((w >> 2) * 4 * nl + (w & 3)) * 1024, rot = idx & 15;
  v4u a[WIN];
#pragma unroll
  for (int i = 0; i < KA; ++i) { a[i] = bl128<AUX_NT>(rk, voff, piece_off<nl>(wbase, rot, i)); __builtin_amdgcn_sched_barrier(0); }
  Tags tg, tq;
  if (w < 4) {
    float s1 = 0.f, s2 = 0.f;
#pragma unroll
    for (int i = 0; i < 5; ++i) { s1 = dot2(xr[i], ONE2, s1); s2 = dot2(xr[i], xr[i], s2); }
    float meanb, rden;
    ln_consts(wave_sum(s1), wave_sum(s2), meanb, rden);
#pragma unroll
    for (int i = 0; i < 5; ++i) {
      const int id = lane + 64 * i - 40 * jq;
      const unsigned d2 = pkbf(blo(xr[i]) - meanb, bhi(xr[i]) - meanb);
      const unsigned pk = pkbf(blo(d2) * rden, bhi(d2) * rden);
      if (id >= 0 && id < 40) *(LAS unsigned*)(L + L_XN + w * 160 + id * 4) = pk;
    }
  }
  {
    const unsigned u = __builtin_amdgcn_readfirstlane(ev) + 1u;
    tg.f0 = u & 0xfffu; tg.f1 = (u * 0x2b5u + 0x3a6u) & 0xfffu; tg.f2 = (u * 0x9c7u + 0x51du) & 0xfffu; tg.f3 = (~u) & 0xfffu; tg.mask = 0xfffff000u; tg.low = 0xfffu;
    tq.f0 = tg.f0 & 0xffu; tq.f1 = tg.f1 & 0xffu; tq.f2 = tg.f2 & 0xffu; tq.f3 = tg.f3 & 0xffu; tq.mask = 0xffffff00u; tq.low = 0xffu;
    asm volatile("" : "+v"(tq.mask));
    asm volatile("" : "+s"(tg.f0), "+s"(tg.f1), "+s"(tg.f2), "+s"(tg.f3), "+v"(tg.mask) :: "memory");
  }
  if (w < 4) {
    lds_signal(L, FL_QA, lane);
    lds_wait_ge(L, FL_QA, 4u, deadline);
    s16x4 af[2][5], bf[5];
#pragma unroll
    for (int ks = 0; ks < 5; ++ks) {
      bf[ks] = *(LAS s16x4*)(L + L_XN + (li & 3) * 160 + (16 * ks + 4 * lg) * 2);
#pragma unroll
      for (int tt = 0; tt < 2; ++tt) af[tt][ks] = trd(L, L_WQ + (16 * ks + 4 * lg + (li >> 2)) * 256 + ((2 * w + tt) ^ ((4 * lg + (li >> 2)) & 7)) * 32 + (li & 3) * 8);
    }
    f32x4 c0 = {0.f, 0.f, 0.f, 0.f}, c1 = c0;
#pragma unroll
    for (int ks = 0; ks < 5; ++ks) {
      c0 = __builtin_amdgcn_mfma_f32_16x16x16bf16_1k(af[0][ks], bf[ks], c0, 0, 0, 0);
      c1 = __builtin_amdgcn_mfma_f32_16x16x16bf16_1k(af[1][ks], bf[ks], c1, 0, 0, 0);
    }
    const int qo = li < 4 ? WS_QP + (((h * 4 + li) * 8 + jq) * 32 + 8 * w + lg) * 16 : OOB - 64;
    publish(rw, qo, v4u{enc1(f2u(c0[0]) + 0x80u, tq.f0, tq.mask), enc1(f2u(c0[1]) + 0x80u, tq.f1, tq.mask), enc1(f2u(c0[2]) + 0x80u, tq.f2, tq.mask), enc1(f2u(c0[3]) + 0x80u, tq.f3, tq.mask)});
    publish(rw, qo + 64, v4u{enc1(f2u(c1[0]) + 0x80u, tq.f0, tq.mask), enc1(f2u(c1[1]) + 0x80u, tq.f1, tq.mask), enc1(f2u(c1[2]) + 0x80u, tq.f2, tq.mask), enc1(f2u(c1[3]) + 0x80u, tq.f3, tq.mask)});
  }
#pragma unroll
  for (int i = KA; i < KB; ++i) { a[i] = bl128<AUX_NT>(rk, voff, piece_off<nl>(wbase, rot, i)); __builtin_amdgcn_sched_barrier(0); }
  if (w == 0) {
    int off[4]; v4u c[4];
#pragma unroll
    for (int i = 0; i < 4; ++i) off[i] = WS_QP + ((h * 4 + b) * 256 + 64 * i + lane) * 16;
    wait_until(t_start + QDELAY);
    gather<4>(rw, off, c, tq, deadline);
    constexpr unsigned M24 = 0xffffff00u;
    float q0 = (u2f(c[0].x & M24) + u2f(c[1].x & M24)) + (u2f(c[2].x & M24) + u2f(c[3].x & M24)), q1 = (u2f(c[0].y & M24) + u2f(c[1].y & M24)) + (u2f(c[2].y & M24) + u2f(c[3].y & M24));
    float q2 = (u2f(c[0].z & M24) + u2f(c[1].z & M24)) + (u2f(c[2].z & M24) + u2f(c[3].z & M24)), q3 = (u2f(c[0].w & M24) + u2f(c[1].w & M24)) + (u2f(c[2].w & M24) + u2f(c[3].w & M24));
    q0 = x32_sum(q0); q1 = x32_sum(q1); q2 = x32_sum(q2); q3 = x32_sum(q3);
    if (lane < 32) *(LAS v2u*)(L + L_QF + lane * 8) = v2u{pkbf(q0, q1), pkbf(q2, q3)};
    lds_signal(L, FL_Q, lane);
  }
  int role = 0, strip = 0;
  if constexpr (s) role = 1;
  else if (slot < 14) { const int mm = xp + 4 * (slot >> 1); if (mm < 27) { role = 3; strip = 2 * mm + (slot & 1); } }
  else if (slot < 26) { const int sp = slot - 14, n = xp + 4 * (sp >> 2); if (n < 10) { role = 4; strip = 4 * n + (sp & 3); } }

#pragma unroll
  for (int i = KB; i < WIN; ++i) { a[i] = bl128<AUX_NT>(rk, voff, piece_off<nl>(wbase, rot, i)); __builtin_amdgcn_sched_barrier(0); }
  lds_wait_ge(L, FL_Q, 1u, deadline);
  const v4u q = *(LAS v4u*)(L + L_QF + li * 16);
  float M;
  attend<nl>(rk, rv, voff, wbase, rot, a, q, L, w, lane, M, deadline);
  if (lds_arrive(L, FL_PART, lane) == 7u) {
    LAS char* ow = L + L_OW + (lane >> 5) * 2048 + (lane & 31) * 16;
    const f32x4 oa = *(LAS f32x4*)(ow), ob = *(LAS f32x4*)(ow + 512), oc = *(LAS f32x4*)(ow + 1024), od = *(LAS f32x4*)(ow + 1536);
    const f32x4 la = *(LAS f32x4*)(L + L_ML + 32), lb = *(LAS f32x4*)(L + L_ML + 48);
    float o[4];
#pragma unroll
    for (int k = 0; k < 4; ++k) o[k] = x32_sum((oa[k] + ob[k]) + (oc[k] + od[k]));
    const float lsum = ((la[0] + la[1]) + (la[2] + la[3])) + ((lb[0] + lb[1]) + (lb[2] + lb[3]));
    const float now = u2f((unsigned)__builtin_amdgcn_s_memrealtime() << 12);
    const v4u ch = enc4(lane < 32 ? o[0] : (lane == 32 ? M : now), lane < 32 ? o[1] : lsum, o[2], o[3], tg);
    const int base = WS_OP + (h * 8 + jq) * 640;
    publish(rw, lane <= 32 ? base + lane * 16 : (lane == 33 ? WS_B + g * 16 : OOB), ch);
    lds_signal(L, FL_PUB, lane);
  }
  [&]() {
  if (role == 0) return;
  const int gl = lg * 64 + (li & 3) * 16, bt = li >> 2;
  if (w < 4) {
    if constexpr (s) {
      lds_wait_ge(L, FL_V, 4u, deadline);
#pragma unroll
      for (int i = 0; i < 11; ++i) {
        const int n = w * 11 + i, id = n * 64 + lane, r = id / 22, c = min(id - r * 22, 19);
        dma16(p.wo + (size_t)(128 * h + r) * 1280 + b * 320 + c * 16, Lb + L_IMG + n * 1024);
      }
    } else if (role == 3) {
      if (E3) lds_wait_ge(L, FL_V, 4u, deadline);
#pragma unroll
      for (int i = 0; i < 20; ++i) {
        if (i == E3) lds_wait_ge(L, FL_PUB, 1u, deadline);
        const int n = 4 * i + w, r = n * 8 + (lane >> 3);
        const int c = ((((lane & 7) >> 1) ^ (((r >> 1) & 1) | ((r >> 3) & 2))) << 1) | (lane & 1);
        dma16(((c & 4) ? p.w3 : p.w1) + (size_t)r * 3456 + strip * 64 + (c & 3) * 16, Lb + L_IMG + n * 1024);
      }
    } else {
      if (E4) lds_wait_ge(L, FL_V, 4u, deadline);
#pragma unroll
      for (int i = 0; i < 14; ++i) {
        if (i == E4) lds_wait_ge(L, FL_PUB, 1u, deadline);
        const int n = w + 4 * i, rp = lane >> 1, r = n * 32 + (rp ^ ((rp >> 2) & 4));
        if (n < 54) dma16(p.w2 + (size_t)r * 1280 + strip * 32 + (lane & 1) * 16, Lb + L_IMG + n * 1024);
      }
    }
    if constexpr (!s) lds_signal(L, FL_DI, lane);
    VM_DRAIN();
  } else if (!s && w == 4) {
    lds_wait_ge(L, FL_DI, 4u, deadline);
    post_tlast<2>(L, rw, lane, tg, t_start, deadline);
  }
  if constexpr (s) {
    v4u af[12];
    int apo[3];
    if (w < 4) {
      lds_signal(L, FL_DMA, lane); lds_wait_ge(L, FL_DMA, 4u, deadline);
      const int nt = w < 2 ? 3 : 2, t0 = w < 2 ? 3 * w : 2 * w + 2;
#pragma unroll
      for (int tt = 0; tt < 3; ++tt) {
        const int t = (tt < nt) ? t0 + tt : t0;
#pragma unroll
        for (int ks = 0; ks < 4; ++ks) af[tt * 4 + ks] = trfrag(L, L_IMG + (32 * ks + 4 * lg + (li >> 2)) * 352 + (16 * t + 4 * (li & 3)) * 2, 16 * 352);
        apo[tt] = (li < 4 && tt < nt) ? WS_AP + ((h * 4 + b) * 4 + li) * 640 + (4 * t + lg) * 16 : OOB;
      }
    } else {
      const int bb = w - 4, half = lane >> 5;
      int off[2]; v4u c[2];
      off[0] = WS_OP + (h * 8 + 2 * bb + half) * 640 + (lane & 31) * 16;
      off[1] = WS_OP + (h * 8 + 2 * bb + half) * 640 + 512;
      gather<2>(rw, off, c, tg, deadline);
      const float ms = dec(c[1].x), ls = dec(c[1].y);
      const float Mx = x32_max(ms), e = __builtin_amdgcn_exp2f((ms - Mx) * L2E), wgt = e * __builtin_amdgcn_rcpf(x32_sum(e * ls));
      const float o0 = x32_sum(wgt * dec(c[0].x)), o1 = x32_sum(wgt * dec(c[0].y)), o2 = x32_sum(wgt * dec(c[0].z)), o3 = x32_sum(wgt * dec(c[0].w));
      if (lane < 32) *(LAS v2u*)(L + L_OB + bb * 256 + (lane >> 3) * 64 + (lane & 3) * 16 + ((lane >> 2) & 1) * 8) = v2u{pkbf(o0, o1), pkbf(o2, o3)};
    }
    wg_barrier();
    if (w < 4) {
      v4u bfr[4];
#pragma unroll
      for (int ks = 0; ks < 4; ++ks) bfr[ks] = *(LAS v4u*)(L + L_OB + (li & 3) * 256 + ks * 64 + lg * 16);
      f32x4 c[3];
#pragma unroll
      for (int tt = 0; tt < 3; ++tt) c[tt] = f32x4{0.f, 0.f, 0.f, 0.f};
#pragma unroll
      for (int ks = 0; ks < 4; ++ks)
#pragma unroll
        for (int tt = 0; tt < 3; ++tt) c[tt] = mfma32(af[tt * 4 + ks], bfr[ks], c[tt]);
#pragma unroll
      for (int tt = 0; tt < 3; ++tt) publish(rw, apo[tt], enc4(c[tt][0], c[tt][1], c[tt][2], c[tt][3], tg));
    } else if (w < 7) {
      const int cc = 2 * (w - 4) + (lane >> 5);
      const bool on = cc < 5, mine = on && (lane & 31) == 0;
      const rsrc_t rx = mkrs(p.x, 4 * 1280);
      const v2u xx = __builtin_amdgcn_raw_buffer_load_b64(rx, mine ? b * 1280 + (20 * h + 4 * cc) * 2 : OOB, 0, 0);
      int off[1]; v4u c[1];
      off[0] = on ? WS_AP + (((lane & 31) * 4 + (h >> 3)) * 4 + b) * 640 + (5 * (h & 7) + cc) * 16 : OOB;
      wait_until(__builtin_amdgcn_s_memrealtime() + (D1 - 4));
      gather<1>(rw, off, c, tg, deadline);
      const unsigned r01 = pkbf(x16_sum(row16_sum(dec(c[0].x))), x16_sum(row16_sum(dec(c[0].y))));
      const unsigned r23 = pkbf(x16_sum(row16_sum(dec(c[0].z))), x16_sum(row16_sum(dec(c[0].w))));
      const unsigned h01 = pkbf(blo(xx.x) + blo(r01), bhi(xx.x) + bhi(r01)), h23 = pkbf(blo(xx.y) + blo(r23), bhi(xx.y) + bhi(r23));
      publish(rw, mine ? WS_H + (b * 160 + 5 * h + cc) * 16 : OOB, encp(h01, h23, tg));
    }
  } else if (role == 3) {
    wg_barrier();
    const int pt = w & 1, kq = w >> 1, np = kq < 2 ? 3 : 2;
    v4u ag[6], au[6];
    f32x4 sg = {0.f, 0.f, 0.f, 0.f}, su = sg;
#pragma unroll
    for (int i = 0; i < 6; ++i) {
      const int pp = (i >> 1) < np ? kq + 4 * (i >> 1) : kq;
      const int ro = L_IMG + (64 * pp + 16 * lg + 8 * (i & 1) + (li >> 2)) * 128 + (li & 3) * 8, phi = ((li >> 3) & 1) | ((lg & 1) << 1);
      ag[i] = trfrag(L, ro + (pt ^ phi) * 32, 4 * 128); au[i] = trfrag(L, ro + ((2 + pt) ^ phi) * 32, 4 * 128);
      if ((i >> 1) < np) { sg = mfma32(ag[i], v4u{ONE2, ONE2, ONE2, ONE2}, sg); su = mfma32(au[i], v4u{ONE2, ONE2, ONE2, ONE2}, su); }
    }
    if (li == 0) {
      *(LAS f32x4*)(L + L_RED + (w * 2 + 0) * 320 + lg * 80 + 64) = sg;
      *(LAS f32x4*)(L + L_RED + (w * 2 + 1) * 320 + lg * 80 + 64) = su;
    }
    int off[3]; v4u c[3];
#pragma unroll
    for (int i = 0; i < 3; ++i) off[i] = i < np ? WS_H + bt * 2560 + (kq + 4 * i) * 256 + gl : OOB;
    wait_due(L, D2, deadline);
    gather<3>(rw, off, c, tg, deadline);
    f32x4 cg = {0.f, 0.f, 0.f, 0.f}, cu = cg, cg2 = cg, cu2 = cg;
    float s1 = 0.f, s2 = 0.f;
#pragma unroll
    for (int i = 0; i < 3; ++i) {
      if (i < np) {
        const unsigned p0 = pack2(c[i].x, c[i].y), p1 = pack2(c[i].z, c[i].w);
        const v4u b0 = {qbc<0>(p0), qbc<0>(p1), qbc<1>(p0), qbc<1>(p1)};
        const v4u b1 = {qbc<2>(p0), qbc<2>(p1), qbc<3>(p0), qbc<3>(p1)};
        cg = mfma32(ag[2 * i], b0, cg); cu = mfma32(au[2 * i], b0, cu);
        cg2 = mfma32(ag[2 * i + 1], b1, cg2); cu2 = mfma32(au[2 * i + 1], b1, cu2);
        s1 = dot2(p0, ONE2, s1); s2 = dot2(p0, p0, s2); s1 = dot2(p1, ONE2, s1); s2 = dot2(p1, p1, s2);
      }
    }
    s1 += dpp<0xB1>(s1); s2 += dpp<0xB1>(s2); s1 += dpp<0x4E>(s1); s2 += dpp<0x4E>(s2);
    s1 = x32_sum(x16_sum(s1)); s2 = x32_sum(x16_sum(s2));
    if ((li & 3) == 0) {
      *(LAS f32x4*)(L + L_RED + (w * 2 + 0) * 320 + lg * 80 + bt * 16) = cg + cg2;
      *(LAS f32x4*)(L + L_RED + (w * 2 + 1) * 320 + lg * 80 + bt * 16) = cu + cu2;
      if (lane < 16 && pt == 0) *(LAS v2u*)(L + L_ST + bt * 32 + kq * 8) = v2u{f2u(s1), f2u(s2)};
    }
    wg_barrier();
    if (w == 0) {
      const int gu = lane >> 5, p2 = (lane >> 4) & 1, eb = (lane >> 2) & 3, eg = lane & 3;
      LAS char* rb_ = L + L_RED + (p2 * 2 + gu) * 320 + eg * 80;
      const f32x4 d0 = *(LAS f32x4*)(rb_ + eb * 16), d1 = *(LAS f32x4*)(rb_ + eb * 16 + 1280), d2 = *(LAS f32x4*)(rb_ + eb * 16 + 2560), d3 = *(LAS f32x4*)(rb_ + eb * 16 + 3840);
      const f32x4 c0 = *(LAS f32x4*)(rb_ + 64), c1 = *(LAS f32x4*)(rb_ + 64 + 1280), c2 = *(LAS f32x4*)(rb_ + 64 + 2560), c3 = *(LAS f32x4*)(rb_ + 64 + 3840);
      const f32x4 sa = *(LAS f32x4*)(L + L_ST + eb * 32), sb = *(LAS f32x4*)(L + L_ST + eb * 32 + 16);
      float meanb, rden;
      ln_consts((sa[0] + sa[2]) + (sb[0] + sb[2]), (sa[1] + sa[3]) + (sb[1] + sb[3]), meanb, rden);
      const f32x4 ds = (d0 + d1) + (d2 + d3), cs = (c0 + c1) + (c2 + c3);
      const unsigned v01 = pkbf(rden * fmaf(-meanb, cs[0], ds[0]), rden * fmaf(-meanb, cs[1], ds[1]));
      const unsigned v23 = pkbf(rden * fmaf(-meanb, cs[2], ds[2]), rden * fmaf(-meanb, cs[3], ds[3]));
      const auto x01 = __builtin_amdgcn_permlane32_swap(v01, v01, false, false), x23 = __builtin_amdgcn_permlane32_swap(v23, v23, false, false);
      unsigned ap[2];
#pragma unroll
      for (int k = 0; k < 2; ++k) {
        const unsigned gp = k ? x23[0] : x01[0], upk = k ? x23[1] : x01[1];
        const float ga = blo(gp), gb = bhi(gp);
        const unsigned sp = pkbf(ga * __builtin_amdgcn_rcpf(1.0f + __builtin_amdgcn_exp2f(-ga * L2E)), gb * __builtin_amdgcn_rcpf(1.0f + __builtin_amdgcn_exp2f(-gb * L2E)));
        ap[k] = pkbf(blo(sp) * blo(upk), bhi(sp) * bhi(upk));
      }
      publish(rw, lane < 32 ? WS_ACT + (eb * 432 + 8 * strip + 4 * p2 + eg) * 16 : OOB, encp(ap[0], ap[1], tg));
    }
  } else {
    wg_barrier();
    const int np = w < 3 ? 4 : 3;
    v4u af[8];
#pragma unroll
    for (int i = 0; i < 8; ++i) {
      const int pp = (i >> 1) < np ? w + 8 * (i >> 1) : w;
      const int r0 = 16 * lg + 8 * (i & 1) + (li >> 2), fl = (lg & 1) << 2;
      const v2u a0 = __builtin_bit_cast(v2u, trd(L, L_IMG + (64 * pp + (r0 ^ fl)) * 32 + (li & 3) * 8)), a1 = __builtin_bit_cast(v2u, trd(L, L_IMG + (64 * pp + ((r0 + 4) ^ fl)) * 32 + (li & 3) * 8));
      af[i] = v4u{a0.x, a0.y, a1.x, a1.y};
    }
    v4u hc[1];
    if (w == 0) {
      int off[1]; off[0] = lane < 16 ? WS_H + ((lane >> 2) * 160 + 4 * strip + (lane & 3)) * 16 : OOB;
      wait_due(L, D2, deadline);
      gather<1>(rw, off, hc, tg, deadline);
    }
    int off[4]; v4u c[4];
#pragma unroll
    for (int i = 0; i < 4; ++i) off[i] = i < np ? WS_ACT + bt * 6912 + (w + 8 * i) * 256 + gl : OOB;
    wait_due(L, D3, deadline);
    gather<4>(rw, off, c, tg, deadline);
    f32x4 cc = {0.f, 0.f, 0.f, 0.f}, cd = cc;
#pragma unroll
    for (int i = 0; i < 4; ++i) {
      if (i < np) {
        const unsigned p0 = pack2(c[i].x, c[i].y), p1 = pack2(c[i].z, c[i].w);
        cc = mfma32(af[2 * i], v4u{qbc<0>(p0), qbc<0>(p1), qbc<1>(p0), qbc<1>(p1)}, cc);
        cd = mfma32(af[2 * i + 1], v4u{qbc<2>(p0), qbc<2>(p1), qbc<3>(p0), qbc<3>(p1)}, cd);
      }
    }
    if ((li & 3) == 0) *(LAS f32x4*)(L + L_RED + w * 256 + (bt * 4 + lg) * 16) = cc + cd;
    wg_barrier();
    if (w == 0) {
      LAS char* rb_ = L + L_RED + (lane & 15) * 16;
      const f32x4 r0 = *(LAS f32x4*)(rb_), r1 = *(LAS f32x4*)(rb_ + 256), r2 = *(LAS f32x4*)(rb_ + 512), r3 = *(LAS f32x4*)(rb_ + 768);
      const f32x4 r4 = *(LAS f32x4*)(rb_ + 1024), r5 = *(LAS f32x4*)(rb_ + 1280), r6 = *(LAS f32x4*)(rb_ + 1536), r7 = *(LAS f32x4*)(rb_ + 1792);
      const f32x4 sm = ((r0 + r1) + (r2 + r3)) + ((r4 + r5) + (r6 + r7));
      const unsigned m01 = pkbf(sm[0], sm[1]), m23 = pkbf(sm[2], sm[3]);
      const v2u o = {pkbf(dec(hc[0].x) + blo(m01), dec(hc[0].y) + bhi(m01)), pkbf(dec(hc[0].z) + blo(m23), dec(hc[0].w) + bhi(m23))};
      const rsrc_t ro = mkrs(p.out, 4 * 1280);
      __builtin_amdgcn_raw_buffer_store_b64(o, ro, lane < 16 ? ((lane >> 2) & 3) * 1280 + (16 * strip + 4 * (lane & 3)) * 2 : OOB, 0, 0);
      if (strip == 0) __builtin_amdgcn_raw_buffer_store_b32(__builtin_amdgcn_readfirstlane(ev) + 1u, rw, lane == 0 ? WS_E : OOB, 0, AUX_SC1);
    }
  }
  }();
}

__global__ void __launch_bounds__(512, 2) fused_block(Params p) {
  extern __shared__ __attribute__((aligned(16))) char smem[];
  if (blockIdx.x & 1) body<1>(p, (LAS char*)smem);
  else body<0>(p, (LAS char*)smem);
}
}

extern "C" void kernel_launch(void* const* d_in, const int* in_sizes, int n_in, void* d_out, int out_size, void* d_ws, size_t ws_size,
                              hipStream_t stream) {
  (void)in_sizes; (void)n_in; (void)out_size; (void)ws_size;
  fk::Params p;
  p.x = (const char*)d_in[0]; p.k = (const char*)d_in[1]; p.v = (const char*)d_in[2]; p.wq = (const char*)d_in[3]; p.wo = (const char*)d_in[4];
  p.w1 = (const char*)d_in[6]; p.w3 = (const char*)d_in[7]; p.w2 = (const char*)d_in[8];
  p.out = (char*)d_out; p.ws = (char*)d_ws;
  (void)hipFuncSetAttribute((const void*)fk::fused_block, hipFuncAttributeMaxDynamicSharedMemorySize, fk::L_END);
  fk::fused_block<<<256, 512, fk::L_END, stream>>>(p);
}
```
